```python
import torch

def _softmax(x, axis=-1):
    return torch.softmax(x, dim=axis)
def _gelu(x):
    return torch.nn.functional.gelu(x, approximate="tanh")
def _silu(x):
    return torch.nn.functional.silu(x)
def _sigmoid(x):
    return torch.sigmoid(x)
def _layernorm(x, axis=-1, eps=1e-5):
    m = x.mean(dim=axis, keepdim=True)
    v = x.var(dim=axis, keepdim=True, unbiased=False)
    return (x - m) / torch.sqrt(v + eps)
def _scatter_add(tgt, idx, src, axis=0):
    return tgt.clone().index_add_(axis, idx, src)
def _segment_sum(src, seg, n):
    out = torch.zeros((n,) + src.shape[1:], dtype=src.dtype)
    return out.index_add_(0, seg, src)
def _pad(x, pw):
    flat = tuple(v for pair in reversed(pw) for v in pair)
    return torch.nn.functional.pad(x, flat)

kernel_name = 'parallel_sum__linear_kernel_hedgehog__dense_activation_silu__prompt'

def setup_inputs():

    _w = (lambda a: (lambda t: t.to(torch.bfloat16) if t.is_floating_point() else t)(torch.as_tensor(a)))
    def _A_setup():
        B, S, D, H = 1, 2048, 576, 32
        Dh = D // H
        return {
            "x": _w((lambda *s: torch.randn(*s, dtype=torch.bfloat16) * 0.05)(B, S, D)),
            "wqkv": _w((lambda *s: torch.randn(*s, dtype=torch.bfloat16) * 0.2)(D, 3 * D)),
            "w_phi": _w((lambda *s: torch.randn(*s, dtype=torch.bfloat16) * 0.2)(H, Dh, Dh) * (Dh ** -0.5)),
            "wo": _w((lambda *s: torch.randn(*s, dtype=torch.bfloat16) * 0.05)(D, D)),
        }
    _A_in = _A_setup()

    def _B_setup():
        B, S, D, F = 1, 2048, 576, 4608
        return {
            "x": _w((lambda *s: torch.randn(*s, dtype=torch.bfloat16) * 0.05)(B, S, D)),
            "w1": _w((lambda *s: torch.randn(*s, dtype=torch.bfloat16) * 1.6)(D, F) * (D ** -0.5)),
            "w2": _w((lambda *s: torch.randn(*s, dtype=torch.bfloat16) * 0.1)(F, D) * (F ** -0.5)),
        }
    _B_in = _B_setup()

    return {
        "A_x": _A_in["x"],
        "A_wqkv": _A_in["wqkv"],
        "A_w_phi": _A_in["w_phi"],
        "A_wo": _A_in["wo"],
        "B_x": _B_in["x"],
        "B_w1": _B_in["w1"],
        "B_w2": _B_in["w2"],
    }

def reference(A_x, A_wqkv, A_w_phi, A_wo, B_x, B_w1, B_w2):
    def _A_ref(x, wqkv, w_phi, wo):
        B, S, D = x.shape
        H = 32; Dh = D // H
        qkv = (lambda x, *d: x.permute(*d))((x @ wqkv).reshape(B, S, 3, H, Dh), 2, 0, 3, 1, 4)
        q, k, v = qkv[0], qkv[1], qkv[2]
        pq = torch.einsum("bhsd,hdf->bhsf", q, w_phi)
        pk = torch.einsum("bhsd,hdf->bhsf", k, w_phi)
        phi_q = torch.concatenate([_softmax(pq, axis=-1), _softmax(-pq, axis=-1)], axis=-1)
        phi_k = torch.concatenate([_softmax(pk, axis=-1), _softmax(-pk, axis=-1)], axis=-1)
        kv = torch.einsum("bhsf,bhsd->bhfd", phi_k, v)
        z  = torch.einsum("bhsf->bhf", phi_k)
        num = torch.einsum("bhqf,bhfd->bhqd", phi_q, kv)
        den = torch.einsum("bhqf,bhf->bhq", phi_q, z)[..., None] + 1e-6
        o = num / den
        return (lambda x, *d: x.permute(*d))(o, 0, 2, 1, 3).reshape(B, S, D) @ wo

    def _B_ref(x, w1, w2):
        h = torch.einsum("bsd,df->bsf", x, w1)
        h = _silu(h)
        out = torch.einsum("bsf,fd->bsd", h, w2)
        return out

    x = A_x
    xn = _layernorm(x, axis=-1)
    return x + _A_ref(xn, A_wqkv, A_w_phi, A_wo) + _B_ref(xn, B_w1, B_w2)
```

```cpp
#include <hip/hip_runtime.h>
#include <hip/hip_bf16.h>

typedef unsigned short u16;
typedef __attribute__((ext_vector_type(8))) __bf16 bf16x8;
typedef __attribute__((ext_vector_type(4))) short s16x4;
typedef __attribute__((ext_vector_type(4))) float f32x4;
typedef __attribute__((ext_vector_type(4))) unsigned u32x4;
typedef __attribute__((ext_vector_type(2))) unsigned u32x2;
#define AS1 __attribute__((address_space(1)))
#define AS3 __attribute__((address_space(3)))

constexpr int S = 2048, D = 576, H = 32, DH = 18, F = 4608;
constexpr int NQKV = 3 * D;
constexpr int KHO = F + D;
constexpr int NFEAT = 2 * DH;
constexpr int KVSZ = NFEAT * DH + NFEAT;

__device__ __forceinline__ float bf2f(u16 v) { return __uint_as_float(((unsigned)v) << 16); }
__device__ __forceinline__ u16 f2bf(float f) { __bf16 b = (__bf16)f; return __builtin_bit_cast(u16, b); }
__device__ __forceinline__ unsigned pack2(float lo, float hi) { return (unsigned)f2bf(lo) | ((unsigned)f2bf(hi) << 16); }
__device__ __forceinline__ float bfr(float f) { return bf2f(f2bf(f)); }

__global__ __launch_bounds__(256) void ln_kernel(const u16* __restrict__ x, u16* __restrict__ xn, float* __restrict__ kvacc) {
  const int wid = threadIdx.x >> 6, lane = threadIdx.x & 63;
  const int row = blockIdx.x * 4 + wid;
  if (blockIdx.x == 0) { for (int i = threadIdx.x; i < H * KVSZ; i += 256) kvacc[i] = 0.f; }
  const u16* xr = x + (size_t)row * D;
  u32x4 c0 = *(const u32x4*)(xr + lane * 8);
  u32x4 c1 = {0, 0, 0, 0};
  if (lane < 8) c1 = *(const u32x4*)(xr + (64 + lane) * 8);
  float v[16];
  for (int i = 0; i < 4; ++i) { v[2 * i] = __uint_as_float(c0[i] << 16); v[2 * i + 1] = __uint_as_float(c0[i] & 0xffff0000u); }
  for (int i = 0; i < 4; ++i) { v[8 + 2 * i] = __uint_as_float(c1[i] << 16); v[8 + 2 * i + 1] = __uint_as_float(c1[i] & 0xffff0000u); }
  float s = 0.f; for (int i = 0; i < 16; ++i) s += v[i];
  for (int o = 32; o > 0; o >>= 1) s += __shfl_xor(s, o);
  const float mean = s * (1.f / D);
  float q = 0.f;
  for (int i = 0; i < 8; ++i) { float d = v[i] - mean; q += d * d; }
  if (lane < 8) for (int i = 8; i < 16; ++i) { float d = v[i] - mean; q += d * d; }
  for (int o = 32; o > 0; o >>= 1) q += __shfl_xor(q, o);
  const float rstd = rsqrtf(q * (1.f / D) + 1e-5f);
  u32x4 o0, o1;
  for (int i = 0; i < 4; ++i) o0[i] = pack2((v[2 * i] - mean) * rstd, (v[2 * i + 1] - mean) * rstd);
  for (int i = 0; i < 4; ++i) o1[i] = pack2((v[8 + 2 * i] - mean) * rstd, (v[8 + 2 * i + 1] - mean) * rstd);
  u16* yr = xn + (size_t)row * D;
  *(u32x4*)(yr + lane * 8) = o0;
  if (lane < 8) *(u32x4*)(yr + (64 + lane) * 8) = o1;
}

__device__ __forceinline__ int sBswz(int r) { return (((r >> 1) & 1) << 1) | ((r >> 3) & 1); }

template <int WM, int WN, int MB, int NB, int EPI>
__global__ __launch_bounds__(WM* WN * 64) void gemm_kernel(const u16* __restrict__ A, int lda, const u16* __restrict__ B1, const u16* __restrict__ B2, int ldb,
                                                           int ktsplit, void* __restrict__ C, int ldc, int ntn, int ksteps_total, int nsplit, size_t slab) {
  constexpr int BM = WM * MB * 16, BN = WN * NB * 16, NW = WM * WN;
  constexpr int ABYTES = BM * 128, BBYTES = BN * 128, STAGE = ABYTES + BBYTES;
  constexpr int APIECES = BM / 8, BPIECES = BN / 8, PIECES = APIECES + BPIECES;
  extern __shared__ __attribute__((aligned(16))) char smem[];
  const int tid = threadIdx.x, lane = tid & 63, wid = tid >> 6;
  const int wm = wid / WN, wn = wid % WN;
  const int bid = blockIdx.x;
  const int split = bid % nsplit, tile = bid / nsplit;
  const int tm = tile / ntn, tn = tile % ntn;
  const int row0 = tm * BM, col0 = tn * BN;
  const int kt0 = (ksteps_total * split) / nsplit, kt1 = (ksteps_total * (split + 1)) / nsplit;
  const int nk = kt1 - kt0;

  auto stage = [&](int kt, int buf) {
    char* base = smem + buf * STAGE;
    const u16* Bp = (kt < ktsplit) ? (B1 + (size_t)kt * 64 * ldb) : (B2 + (size_t)(kt - ktsplit) * 64 * ldb);
#pragma unroll
    for (int p = 0; p < (PIECES + NW - 1) / NW; ++p) {
      const int piece = p * NW + wid;
      if (piece < PIECES) {
        const int r8 = lane >> 3, c = lane & 7;
        const u16* src;
        if (piece < APIECES) {
          const int r = piece * 8 + r8;
          src = A + (size_t)(row0 + r) * lda + kt * 64 + ((c ^ r8) << 3);
        } else {
          const int pb = piece - APIECES, sub = pb >> 3, j = pb & 7;
          const int r = j * 8 + r8;
          const int chunk = (((c >> 1) ^ sBswz(r)) << 1) | (c & 1);
          src = Bp + (size_t)r * ldb + col0 + sub * 64 + (chunk << 3);
        }
        __builtin_amdgcn_global_load_lds((const AS1 void*)src, (AS3 void*)(base + piece * 1024), 16, 0, 0);
      }
    }
  };

  f32x4 acc[MB][NB];
#pragma unroll
  for (int i = 0; i < MB; ++i)
#pragma unroll
    for (int j = 0; j < NB; ++j) acc[i][j] = (f32x4){0.f, 0.f, 0.f, 0.f};

  const int fr = lane & 15, g = lane >> 4, q = (lane & 15) >> 2, p4 = lane & 3;

  stage(kt0, 0);
  asm volatile("s_waitcnt vmcnt(0)" ::: "memory");
  __syncthreads();
  for (int it = 0; it < nk; ++it) {
    if (it + 1 < nk) stage(kt0 + it + 1, (it + 1) & 1);
    const char* Ab = smem + (it & 1) * STAGE;
    const char* Bb = Ab + ABYTES;
#pragma unroll
    for (int kk = 0; kk < 2; ++kk) {
      bf16x8 af[MB];
#pragma unroll
      for (int mb = 0; mb < MB; ++mb) {
        const int r = wm * MB * 16 + mb * 16 + fr;
        af[mb] = *(const bf16x8*)(Ab + r * 128 + (((4 * kk + g) ^ (r & 7)) << 4));
      }
      bf16x8 bfm[NB];
#pragma unroll
      for (int nb = 0; nb < NB; ++nb) {
        const int n = wn * NB * 16 + nb * 16 + 4 * p4;
        const int sub = n >> 6, chunk = (n & 63) >> 3;
        s16x4 t[2];
#pragma unroll
        for (int tt = 0; tt < 2; ++tt) {
          const int kr = 32 * kk + 8 * g + 4 * tt + q;
          const char* ad = Bb + sub * 8192 + kr * 128 + ((chunk ^ (sBswz(kr) << 1)) << 4) + 8 * (p4 & 1);
          t[tt] = __builtin_amdgcn_ds_read_tr16_b64_v4i16((AS3 s16x4*)ad);
        }
        typedef __attribute__((ext_vector_type(8))) short s16x8;
        s16x8 w = {t[0][0], t[0][1], t[0][2], t[0][3], t[1][0], t[1][1], t[1][2], t[1][3]};
        bfm[nb] = __builtin_bit_cast(bf16x8, w);
      }
#pragma unroll
      for (int mb = 0; mb < MB; ++mb)
#pragma unroll
        for (int nb = 0; nb < NB; ++nb)
          acc[mb][nb] = __builtin_amdgcn_mfma_f32_16x16x32_bf16(bfm[nb], af[mb], acc[mb][nb], 0, 0, 0);
    }
    asm volatile("s_waitcnt vmcnt(0)" ::: "memory");
    __syncthreads();
  }
#pragma unroll
  for (int mb = 0; mb < MB; ++mb) {
    const int m = row0 + wm * MB * 16 + mb * 16 + fr;
#pragma unroll
    for (int nb = 0; nb < NB; ++nb) {
      const int n = col0 + wn * NB * 16 + nb * 16 + 4 * g;
      f32x4 v = acc[mb][nb];
      if (EPI == 0) {
        for (int r = 0; r < 4; ++r) v[r] = v[r] / (1.f + __expf(-v[r]));
      }
      if (EPI == 0 || EPI == 1) {
        u32x2 w = {pack2(v[0], v[1]), pack2(v[2], v[3])};
        *(u32x2*)((u16*)C + (size_t)m * ldc + n) = w;
      } else {
        *(f32x4*)((float*)C + (size_t)split * slab + (size_t)m * ldc + n) = v;
      }
    }
  }
}

__device__ __forceinline__ void feat_map(const float* in18, const float* wphi  , float* phi36) {
  float p[DH];
#pragma unroll
  for (int f = 0; f < DH; ++f) {
    float a = 0.f;
#pragma unroll
    for (int d = 0; d < DH; ++d) a = fmaf(in18[d], wphi[d * DH + f], a);
    p[f] = bfr(a);
  }
  float mx = p[0], mn = p[0];
#pragma unroll
  for (int f = 1; f < DH; ++f) { mx = fmaxf(mx, p[f]); mn = fminf(mn, p[f]); }
  float s1 = 0.f, s2 = 0.f;
#pragma unroll
  for (int f = 0; f < DH; ++f) {
    float e1 = __expf(p[f] - mx), e2 = __expf(mn - p[f]);
    phi36[f] = e1; phi36[DH + f] = e2; s1 += e1; s2 += e2;
  }
  const float r1 = 1.f / s1, r2 = 1.f / s2;
#pragma unroll
  for (int f = 0; f < DH; ++f) { phi36[f] = bfr(phi36[f] * r1); phi36[DH + f] = bfr(phi36[DH + f] * r2); }
}

__global__ __launch_bounds__(256) void kv_kernel(const u16* __restrict__ qkv, const u16* __restrict__ wphi_g, float* __restrict__ kvacc) {
  __shared__ float wphi[DH * DH];
  __shared__ float sphi[256][NFEAT + 1];
  __shared__ float sv[256][DH + 1];
  const int h = blockIdx.x, rb = blockIdx.y, tid = threadIdx.x;
  for (int i = tid; i < DH * DH; i += 256) wphi[i] = bf2f(wphi_g[h * DH * DH + i]);
  __syncthreads();
  const int row = rb * 256 + tid;
  const u16* kr = qkv + (size_t)row * NQKV + D + h * DH;
  const u16* vr = qkv + (size_t)row * NQKV + 2 * D + h * DH;
  float kk[DH], phi[NFEAT];
  for (int d = 0; d < DH; ++d) kk[d] = bf2f(kr[d]);
  feat_map(kk, wphi, phi);
  for (int f = 0; f < NFEAT; ++f) sphi[tid][f] = phi[f];
  for (int d = 0; d < DH; ++d) sv[tid][d] = bf2f(vr[d]);
  __syncthreads();
  for (int o = tid; o < KVSZ; o += 256) {
    float a = 0.f;
    if (o < NFEAT * DH) {
      const int f = o / DH, d = o % DH;
      for (int r = 0; r < 256; ++r) a = fmaf(sphi[r][f], sv[r][d], a);
    } else {
      const int f = o - NFEAT * DH;
      for (int r = 0; r < 256; ++r) a += sphi[r][f];
    }
    atomicAdd(&kvacc[h * KVSZ + o], a);
  }
}

__global__ __launch_bounds__(256) void o_kernel(const u16* __restrict__ qkv, const u16* __restrict__ wphi_g, const float* __restrict__ kvacc, u16* __restrict__ ho) {
  __shared__ float wphi[4][DH * DH];
  __shared__ float skv[4][KVSZ];
  const int tid = threadIdx.x, hl = tid >> 6, lane = tid & 63;
  const int h = blockIdx.x * 4 + hl, row = blockIdx.y * 64 + lane;
  for (int i = lane; i < DH * DH; i += 64) wphi[hl][i] = bf2f(wphi_g[h * DH * DH + i]);
  for (int i = lane; i < KVSZ; i += 64) skv[hl][i] = bfr(kvacc[h * KVSZ + i]);
  __syncthreads();
  const u16* qr = qkv + (size_t)row * NQKV + h * DH;
  float qq[DH], phi[NFEAT];
  for (int d = 0; d < DH; ++d) qq[d] = bf2f(qr[d]);
  feat_map(qq, wphi[hl], phi);
  float den = 0.f;
  for (int f = 0; f < NFEAT; ++f) den = fmaf(phi[f], skv[hl][NFEAT * DH + f], den);
  den = bfr(bfr(den) + 1e-6f);
  u16* orow = ho + (size_t)row * KHO + F + h * DH;
  for (int d = 0; d < DH; ++d) {
    float a = 0.f;
    for (int f = 0; f < NFEAT; ++f) a = fmaf(phi[f], skv[hl][f * DH + d], a);
    orow[d] = f2bf(bfr(a) / den);
  }
}

__global__ __launch_bounds__(256) void final_kernel(const u16* __restrict__ x, const float* __restrict__ part, u16* __restrict__ out, int nsplit) {
  const int i = (blockIdx.x * 256 + threadIdx.x) * 8;
  if (i >= S * D) return;
  u32x4 xv = *(const u32x4*)(x + i);
  float a[8];
  for (int j = 0; j < 4; ++j) { a[2 * j] = __uint_as_float(xv[j] << 16); a[2 * j + 1] = __uint_as_float(xv[j] & 0xffff0000u); }
  for (int s = 0; s < nsplit; ++s) {
    f32x4 p0 = *(const f32x4*)(part + (size_t)s * S * D + i), p1 = *(const f32x4*)(part + (size_t)s * S * D + i + 4);
    for (int j = 0; j < 4; ++j) { a[j] += p0[j]; a[4 + j] += p1[j]; }
  }
  u32x4 o;
  for (int j = 0; j < 4; ++j) o[j] = pack2(a[2 * j], a[2 * j + 1]);
  *(u32x4*)(out + i) = o;
}

extern "C" void kernel_launch(void* const* d_in, const int* in_sizes, int n_in, void* d_out, int out_size, void* d_ws, size_t ws_size,
                              hipStream_t stream) {
  const u16* x = (const u16*)d_in[0];
  const u16* wqkv = (const u16*)d_in[1];
  const u16* wphi = (const u16*)d_in[2];
  const u16* wo = (const u16*)d_in[3];
  const u16* w1 = (const u16*)d_in[5];
  const u16* w2 = (const u16*)d_in[6];
  u16* out = (u16*)d_out;
  char* ws = (char*)d_ws;
  size_t off = 0;
  auto take = [&](size_t bytes) { char* p = ws + off; off += (bytes + 255) & ~(size_t)255; return p; };
  u16* xn = (u16*)take((size_t)S * D * 2);
  u16* qkv = (u16*)take((size_t)S * NQKV * 2);
  u16* ho = (u16*)take((size_t)S * KHO * 2);
  float* kvacc = (float*)take((size_t)H * KVSZ * 4);
  constexpr int NSPLIT = 3;
  float* part = (float*)take((size_t)NSPLIT * S * D * 4);

  ln_kernel<<<S / 4, 256, 0, stream>>>(x, xn, kvacc);
  {
    auto kq = gemm_kernel<4, 2, 4, 6, 1>;
    auto kh = gemm_kernel<4, 2, 4, 6, 0>;
    auto k2 = gemm_kernel<4, 2, 2, 6, 2>;
    const int shm1 = 2 * (256 * 128 + 192 * 128), shm2 = 2 * (128 * 128 + 192 * 128);
    hipFuncSetAttribute((const void*)kq, hipFuncAttributeMaxDynamicSharedMemorySize, shm1);
    hipFuncSetAttribute((const void*)kh, hipFuncAttributeMaxDynamicSharedMemorySize, shm1);
    hipFuncSetAttribute((const void*)k2, hipFuncAttributeMaxDynamicSharedMemorySize, shm2);
    kq<<<8 * 9, 512, shm1, stream>>>(xn, D, wqkv, wqkv, NQKV, 9, qkv, NQKV, 9, 9, 1, 0);
    kh<<<8 * 24, 512, shm1, stream>>>(xn, D, w1, w1, F, 9, ho, KHO, 24, 9, 1, 0);
    kv_kernel<<<dim3(H, 8), 256, 0, stream>>>(qkv, wphi, kvacc);
    o_kernel<<<dim3(H / 4, S / 64), 256, 0, stream>>>(qkv, wphi, kvacc, ho);
    k2<<<16 * 3 * NSPLIT, 512, shm2, stream>>>(ho, KHO, w2, wo, D, 72, part, D, 3, 81, NSPLIT, (size_t)S * D);
  }
  final_kernel<<<(S * D / 8 + 255) / 256, 256, 0, stream>>>(x, part, out, NSPLIT);
}
```

```cpp
#include <hip/hip_runtime.h>
#include <hip/hip_bf16.h>

typedef unsigned short u16;
typedef __attribute__((ext_vector_type(8))) __bf16 bf16x8;
typedef __attribute__((ext_vector_type(4))) float f32x4;
typedef __attribute__((ext_vector_type(4))) unsigned u32x4;
typedef __attribute__((ext_vector_type(2))) unsigned u32x2;
#define AS1 __attribute__((address_space(1)))
#define AS3 __attribute__((address_space(3)))
typedef __attribute__((ext_vector_type(2))) float f32x2;
typedef const __attribute__((address_space(4))) float* cfp;

constexpr int S = 2048, D = 576, H = 32, DH = 18, F = 4608;
constexpr int NQKV = 3 * D;
constexpr int NFEAT = 2 * DH;
constexpr int KVSZ = NFEAT * DH + NFEAT;

__device__ __forceinline__ float bf2f(u16 v) { return __uint_as_float(((unsigned)v) << 16); }
__device__ __forceinline__ u16 f2bf(float f) { __bf16 b = (__bf16)f; return __builtin_bit_cast(u16, b); }
__device__ __forceinline__ unsigned pack2(float lo, float hi) { return (unsigned)f2bf(lo) | ((unsigned)f2bf(hi) << 16); }
__device__ __forceinline__ float bfr(float f) { return bf2f(f2bf(f)); }
__device__ __forceinline__ float silu_f(float v) { return v * __builtin_amdgcn_rcpf(1.f + __builtin_amdgcn_exp2f(-1.4426950408889634f * v)); }

#define SBAR() __builtin_amdgcn_sched_barrier(0)
template <int N> __device__ __forceinline__ void waitv() { asm volatile("s_waitcnt vmcnt(%0)" ::"n"(N) : "memory"); }
__device__ __forceinline__ void rd128(u32x4& d, unsigned addr, int off) { asm volatile("ds_read_b128 %0, %1 offset:%2" : "=v"(d) : "v"(addr), "i"(off)); }
__device__ __forceinline__ void rdtr(u32x2& d, unsigned addr, int off) { asm volatile("ds_read_b64_tr_b16 %0, %1 offset:%2" : "=v"(d) : "v"(addr), "i"(off)); }

__global__ __launch_bounds__(256) void ln_kernel(const u16* __restrict__ x, u16* __restrict__ xn, const u16* __restrict__ wphi_g, u16* __restrict__ wA) {
  const int wid = threadIdx.x >> 6, lane = threadIdx.x & 63;
  const int row = blockIdx.x * 4 + wid;
  if (blockIdx.x < 8) {
    for (int i = blockIdx.x * 256 + threadIdx.x; i < H * 2 * 64 * 8; i += 8 * 256) {
      const int j = i & 7, ln = (i >> 3) & 63, fb = (i >> 9) & 1, hd = i >> 10;
      const int f = (ln & 15) + 16 * fb, d = 8 * (ln >> 4) + j;
      wA[i] = (f < DH && d < DH) ? wphi_g[(hd * DH + d) * DH + f] : (u16)0;
    }
  }
  const u16* xr = x + (size_t)row * D;
  u32x4 c0 = *(const u32x4*)(xr + lane * 8);
  u32x4 c1 = {0, 0, 0, 0};
  if (lane < 8) c1 = *(const u32x4*)(xr + (64 + lane) * 8);
  float v[16];
  for (int i = 0; i < 4; ++i) { v[2 * i] = __uint_as_float(c0[i] << 16); v[2 * i + 1] = __uint_as_float(c0[i] & 0xffff0000u); }
  for (int i = 0; i < 4; ++i) { v[8 + 2 * i] = __uint_as_float(c1[i] << 16); v[8 + 2 * i + 1] = __uint_as_float(c1[i] & 0xffff0000u); }
  float s = 0.f; for (int i = 0; i < 16; ++i) s += v[i];
  for (int o = 32; o > 0; o >>= 1) s += __shfl_xor(s, o);
  const float mean = s * (1.f / D);
  float q = 0.f;
  for (int i = 0; i < 8; ++i) { float d = v[i] - mean; q += d * d; }
  if (lane < 8) for (int i = 8; i < 16; ++i) { float d = v[i] - mean; q += d * d; }
  for (int o = 32; o > 0; o >>= 1) q += __shfl_xor(q, o);
  const float rstd = rsqrtf(q * (1.f / D) + 1e-5f);
  u32x4 o0, o1;
  for (int i = 0; i < 4; ++i) o0[i] = pack2((v[2 * i] - mean) * rstd, (v[2 * i + 1] - mean) * rstd);
  for (int i = 0; i < 4; ++i) o1[i] = pack2((v[8 + 2 * i] - mean) * rstd, (v[8 + 2 * i + 1] - mean) * rstd);
  u16* yr = xn + (size_t)row * D;
  *(u32x4*)(yr + lane * 8) = o0;
  if (lane < 8) *(u32x4*)(yr + (64 + lane) * 8) = o1;
}

template <int WM, int WN, int MB, int NB, int BSUB, int NK, int QKVMAP>
__device__ __forceinline__ void gemm_core(const u16* __restrict__ A, int lda, const u16* __restrict__ B, int ldb, char* smem, f32x4 (&acc)[MB][NB]) {
  constexpr int BM = WM * MB * 16, NW = WM * WN;
  constexpr int ABYTES = BM * 128, BBYTES = BSUB * 8192;
  constexpr int BOFF = 3 * ABYTES;
  constexpr int APW = BM / 8 / NW, BPW = BSUB * 8 / NW;
  static_assert((BM / 8) % NW == 0 && (BSUB * 8) % NW == 0, "pieces");
  int tid = threadIdx.x; asm volatile("" : "+v"(tid));
  const int lane = tid & 63, wid = __builtin_amdgcn_readfirstlane(tid >> 6);
  const int wm = wid / WN, wn = wid % WN;
  unsigned aoffs[APW], boffs[BPW];
  {
    const int r8 = lane >> 3, c = lane & 7;
#pragma unroll
    for (int p = 0; p < APW; ++p) {
      const int piece = p * NW + wid;
      aoffs[p] = (unsigned)((piece * 8 + r8) * lda + ((c ^ r8) << 3)) * 2u;
    }
#pragma unroll
    for (int p = 0; p < BPW; ++p) {
      const int pb = p * NW + wid, sub = pb >> 3, j = pb & 7;
      const int r = j * 8 + r8;
      const int swz = (((r >> 1) & 1) << 1) | ((r >> 3) & 1);
      const int chunk = (((c >> 1) ^ swz) << 1) | (c & 1);
      int col = sub * 64 + (chunk << 3);
      if (QKVMAP) col = col < 432 ? (col / 144) * 576 + (col % 144) : 0;
      boffs[p] = (unsigned)(r * ldb + col) * 2u;
    }
  }
  auto stageA1 = [&](int slot, int p, int kt) {
    __builtin_amdgcn_global_load_lds((const AS1 void*)((const char*)(A + kt * 64) + aoffs[p]), (AS3 void*)(smem + slot * ABYTES + (p * NW + wid) * 1024), 16, 0, 0);
  };
  auto stageB1 = [&](int slot, int p, int kt) {
    __builtin_amdgcn_global_load_lds((const AS1 void*)((const char*)(B + (size_t)kt * 64 * ldb) + boffs[p]), (AS3 void*)(smem + BOFF + slot * BBYTES + (p * NW + wid) * 1024), 16, 0, 0);
  };
#pragma unroll
  for (int p = 0; p < BPW; ++p) stageB1(0, p, 0);
#pragma unroll
  for (int p = 0; p < APW; ++p) stageA1(0, p, 0);
#pragma unroll
  for (int p = 0; p < APW; ++p) stageA1(1, p, 1);

  const int fr = lane & 15, g = lane >> 4, q = (lane & 15) >> 2, p4 = lane & 3;
  const unsigned sbase = (unsigned)(size_t)(AS3 char*)smem;
  constexpr bool AIMM = (2 * ABYTES + (MB - 1) * 2048) < 65536;
  unsigned aaddr[2][AIMM ? 1 : 3];
  {
    const int r = wm * MB * 16 + fr;
#pragma unroll
    for (int s = 0; s < (AIMM ? 1 : 3); ++s) {
      aaddr[0][s] = sbase + s * ABYTES + r * 128 + (((0 + g) ^ (r & 7)) << 4);
      aaddr[1][s] = sbase + s * ABYTES + r * 128 + (((4 + g) ^ (r & 7)) << 4);
    }
  }
  unsigned baddr[NB];
  {
    const int sB = (((q >> 1) & 1) << 1) | (g & 1);
    const int kr = 8 * g + q;
#pragma unroll
    for (int nb = 0; nb < NB; ++nb) {
      const int cb = wn * NB + nb;
      baddr[nb] = sbase + BOFF + (cb >> 2) * 8192 + kr * 128 + ((((cb & 3) ^ sB)) << 5) + ((p4 >> 1) << 4) + 8 * (p4 & 1);
    }
  }
#pragma unroll
  for (int i = 0; i < MB; ++i)
#pragma unroll
    for (int j = 0; j < NB; ++j) acc[i][j] = (f32x4){0.f, 0.f, 0.f, 0.f};
  u32x4 af[2][MB];
  u32x2 bt[2][NB][2];
  auto wait_set = [&](int set) {
#pragma unroll
    for (int i = 0; i < MB; ++i) asm volatile("" : "+v"(af[set][i]));
    asm volatile("s_waitcnt lgkmcnt(0)" ::: "memory");
#pragma unroll
    for (int i = 0; i < MB; ++i) asm volatile("" : "+v"(af[set][i]));
#pragma unroll
    for (int j = 0; j < NB; ++j) asm volatile("" : "+v"(bt[set][j][0]), "+v"(bt[set][j][1]));
  };
  constexpr int NRD = 2 * NB + MB;
  constexpr int NRS = NRD < MB * NB ? NRD : MB * NB;
#define PHASE(cs, rs, aslot, bslot, kk, do_reads, sb, sa, kb, ka)                                                                            \
  _Pragma("unroll") for (int i = 0; i < MB * NB; ++i) {                                                                              \
    const int mb = i / NB, nb = i % NB;                                                                                              \
    u32x4 bw = {bt[cs][nb][0][0], bt[cs][nb][0][1], bt[cs][nb][1][0], bt[cs][nb][1][1]};                                            \
    acc[mb][nb] = __builtin_amdgcn_mfma_f32_16x16x32_bf16(__builtin_bit_cast(bf16x8, bw), __builtin_bit_cast(bf16x8, af[cs][mb]), acc[mb][nb], 0, 0, 0); \
    SBAR();                                                                                                                          \
    if (do_reads) {                                                                                                                  \
      _Pragma("unroll") for (int r = (i < NRS ? (i * NRD) / NRS : NRD); r < (i < NRS ? ((i + 1) * NRD) / NRS : NRD); ++r) {          \
        if (r < 2 * NB) rdtr(bt[rs][r >> 1][r & 1], baddr[r >> 1], (bslot) * BBYTES + (kk) * 4096 + (r & 1) * 512);                  \
        else rd128(af[rs][r - 2 * NB], aaddr[kk][AIMM ? 0 : (aslot)], (AIMM ? (aslot) * ABYTES : 0) + (r - 2 * NB) * 2048);                                                       \
        SBAR();                                                                                                                      \
      }                                                                                                                              \
    }                                                                                                                                \
    _Pragma("unroll") for (int s = (i * (APW + BPW)) / (MB * NB); s < ((i + 1) * (APW + BPW)) / (MB * NB); ++s) {                  \
      if (s < BPW) { if ((sb) >= 0) stageB1(sb, s, kb); }                                                                                 \
      else { if ((sa) >= 0) stageA1(sa, s - BPW, ka); }                                                                                 \
      SBAR();                                                                                                                        \
    }                                                                                                                                \
  }
  waitv<APW>();
  __builtin_amdgcn_s_barrier();
#pragma unroll
  for (int nb = 0; nb < NB; ++nb) { rdtr(bt[0][nb][0], baddr[nb], 0); rdtr(bt[0][nb][1], baddr[nb], 512); }
#pragma unroll
  for (int mb = 0; mb < MB; ++mb) rd128(af[0][mb], aaddr[0][0], mb * 2048);
#pragma unroll
  for (int it = 0; it < NK; ++it) {
    wait_set(0);
    SBAR();
    PHASE(0, 1, it % 3, it & 1, 1, true, it == 0 ? 1 : -1, it == 0 ? 2 : -1, 1, 2);
    wait_set(1);
    if (it + 2 < NK) waitv<APW>(); else waitv<0>();
    __builtin_amdgcn_s_barrier();
    SBAR();
    PHASE(1, 0, (it + 1) % 3, (it + 1) & 1, 0, it + 1 < NK, (it + 2 < NK) ? (it & 1) : -1, (it + 3 < NK) ? (it % 3) : -1, it + 2, it + 3);
  }
#undef PHASE
}

__device__ __forceinline__ float red16_32_max(float v) {
  auto r = __builtin_amdgcn_permlane16_swap(__float_as_uint(v), __float_as_uint(v), false, false);
  v = fmaxf(__uint_as_float(r[0]), __uint_as_float(r[1]));
  r = __builtin_amdgcn_permlane32_swap(__float_as_uint(v), __float_as_uint(v), false, false);
  return fmaxf(__uint_as_float(r[0]), __uint_as_float(r[1]));
}
__device__ __forceinline__ float red16_32_add(float v) {
  auto r = __builtin_amdgcn_permlane16_swap(__float_as_uint(v), __float_as_uint(v), false, false);
  v = __uint_as_float(r[0]) + __uint_as_float(r[1]);
  r = __builtin_amdgcn_permlane32_swap(__float_as_uint(v), __float_as_uint(v), false, false);
  return __uint_as_float(r[0]) + __uint_as_float(r[1]);
}
__device__ __forceinline__ void feat_mfma(u32x4 xb, u32x4 wa0, u32x4 wa1, bool g0, float (&e)[8], float (&ex)[4]) {
  f32x4 z = {0.f, 0.f, 0.f, 0.f};
  f32x4 p0 = __builtin_amdgcn_mfma_f32_16x16x32_bf16(__builtin_bit_cast(bf16x8, wa0), __builtin_bit_cast(bf16x8, xb), z, 0, 0, 0);
  f32x4 p1 = __builtin_amdgcn_mfma_f32_16x16x32_bf16(__builtin_bit_cast(bf16x8, wa1), __builtin_bit_cast(bf16x8, xb), z, 0, 0, 0);
  float p[4], q[2];
#pragma unroll
  for (int r = 0; r < 4; ++r) p[r] = bfr(p0[r]);
  q[0] = bfr(p1[0]); q[1] = bfr(p1[1]);
  float mx = fmaxf(fmaxf(p[0], p[1]), fmaxf(p[2], p[3])), mn = fminf(fminf(p[0], p[1]), fminf(p[2], p[3]));
  if (g0) { mx = fmaxf(mx, fmaxf(q[0], q[1])); mn = fminf(mn, fminf(q[0], q[1])); }
  mx = red16_32_max(mx);
  mn = -red16_32_max(-mn);
  float s1 = 0.f, s2 = 0.f;
#pragma unroll
  for (int r = 0; r < 4; ++r) { e[r] = __expf(p[r] - mx); e[4 + r] = __expf(mn - p[r]); s1 += e[r]; s2 += e[4 + r]; }
#pragma unroll
  for (int r = 0; r < 2; ++r) { ex[r] = g0 ? __expf(q[r] - mx) : 0.f; ex[2 + r] = g0 ? __expf(mn - q[r]) : 0.f; s1 += ex[r]; s2 += ex[2 + r]; }
  s1 = red16_32_add(s1);
  s2 = red16_32_add(s2);
  const float r1 = 1.f / s1, r2 = 1.f / s2;
#pragma unroll
  for (int r = 0; r < 4; ++r) { e[r] = bfr(e[r] * r1); e[4 + r] = bfr(e[4 + r] * r2); }
  ex[0] = bfr(ex[0] * r1); ex[1] = bfr(ex[1] * r1); ex[2] = bfr(ex[2] * r2); ex[3] = bfr(ex[3] * r2);
}

__global__ __launch_bounds__(512) void gemm1_kernel(const u16* __restrict__ xn, const u16* __restrict__ w1, const u16* __restrict__ wqkv,
                                                    const u16* __restrict__ wA, u16* __restrict__ h, u16* __restrict__ qbuf, float* __restrict__ kvpart) {
  extern __shared__ __attribute__((aligned(16))) char smem[];
  if (blockIdx.x < 192) {
    const int tm = blockIdx.x / 24, tn = blockIdx.x % 24;
    const int row0 = tm * 256, col0 = tn * 192;
    f32x4 acc[4][6];
    gemm_core<4, 2, 4, 6, 3, 9, 0>(xn + (size_t)row0 * D, D, w1 + col0, F, smem, acc);
    int tid = threadIdx.x; asm volatile("" : "+v"(tid));
    const int lane = tid & 63, wid = tid >> 6, fr = lane & 15, g = lane >> 4;
    const int wm = wid >> 1, wn = wid & 1;
#pragma unroll
    for (int mb = 0; mb < 4; ++mb) {
      const int m = row0 + wm * 64 + mb * 16 + fr;
#pragma unroll
      for (int nb = 0; nb < 6; ++nb) {
        const int n = col0 + wn * 96 + nb * 16 + 4 * g;
        f32x4 v = acc[mb][nb];
        for (int r = 0; r < 4; ++r) v[r] = silu_f(bfr(v[r]));
        u32x2 w = {pack2(v[0], v[1]), pack2(v[2], v[3])};
        *(u32x2*)(h + (size_t)m * F + n) = w;
      }
    }
  } else {
    const int b = blockIdx.x - 192;
    const int tm = b >> 2, tn = b & 3;
    const int row0 = tm * 128;
    f32x4 acc[4][7];
    gemm_core<2, 4, 4, 7, 7, 9, 1>(xn + (size_t)row0 * D, D, wqkv + tn * 144, NQKV, smem, acc);
    int tid = threadIdx.x; asm volatile("" : "+v"(tid));
    const int lane = tid & 63, wid = tid >> 6, fr = lane & 15, g = lane >> 4;
    const int wm = wid >> 2, wn = wid & 3;
    constexpr int TP = 296;
    u16* T = (u16*)smem;
    u16* PH = (u16*)(smem + 75776);
    u16* VV = (u16*)(smem + 75776 + 49152);
    __syncthreads();
#pragma unroll
    for (int mb = 0; mb < 4; ++mb) {
      const int ml = wm * 64 + mb * 16 + fr;
#pragma unroll
      for (int nb = 0; nb < 7; ++nb) {
        const int cb = wn * 7 + nb;
        if (cb < 27) {
          f32x4 v = acc[mb][nb];
          u32x2 w = {pack2(v[0], v[1]), pack2(v[2], v[3])};
          if (cb < 9) *(u32x2*)(qbuf + (size_t)(row0 + ml) * D + tn * 144 + cb * 16 + 4 * g) = w;
          else *(u32x2*)(T + ml * TP + (cb - 9) * 16 + 4 * g) = w;
        }
      }
    }
#pragma unroll 1
    for (int hh = 0; hh < 2; ++hh) {
      __syncthreads();
#pragma unroll
      for (int u = 0; u < 4; ++u) {
        const int unit = wid * 4 + u, hl = unit >> 3, rb = unit & 7;
        const int head = hh * 4 + hl;
        const u16* wa = wA + ((size_t)(tn * 8 + head) * 2 * 64 + lane) * 8;
        const u32x4 wa0 = *(const u32x4*)wa, wa1 = *(const u32x4*)(wa + 64 * 8);
        const unsigned* kr = (const unsigned*)(T + (rb * 16 + fr) * TP + head * DH + 8 * g);
        const u32x4 xb = {kr[0], kr[1], kr[2], kr[3]};
        float e[8], ex[4];
        feat_mfma(xb, wa0, wa1, g == 0, e, ex);
        u16* pr = PH + (hl * 128 + rb * 16 + fr) * 48;
        *(u32x2*)(pr + 4 * g) = (u32x2){pack2(e[0], e[1]), pack2(e[2], e[3])};
        *(u32x2*)(pr + 16 + 4 * g) = (u32x2){pack2(e[4], e[5]), pack2(e[6], e[7])};
        if (g == 0) *(u32x2*)(pr + 32) = (u32x2){pack2(ex[0], ex[1]), pack2(ex[2], ex[3])};
        else *(u32x2*)(pr + 32 + 4 * g) = (u32x2){0u, 0u};
        const unsigned* vr = (const unsigned*)(T + (rb * 16 + fr) * TP + 144 + head * DH);
        u32x4 vv;
        if (g < 2) vv = (u32x4){vr[4 * g], vr[4 * g + 1], vr[4 * g + 2], vr[4 * g + 3]};
        else if (g == 2) vv = (u32x4){vr[8], 0x3f80u, 0u, 0u};
        else vv = (u32x4){0u, 0u, 0u, 0u};
        *(u32x4*)(VV + (hl * 128 + rb * 16 + fr) * 32 + 8 * g) = vv;
      }
      __syncthreads();
      if (wid < 4) {
        const int hl = wid;
        f32x4 a2[3][2];
#pragma unroll
        for (int i = 0; i < 3; ++i)
#pragma unroll
          for (int j = 0; j < 2; ++j) a2[i][j] = (f32x4){0.f, 0.f, 0.f, 0.f};
        const int q4 = (lane & 15) >> 2, p4 = lane & 3;
        const unsigned sb = (unsigned)(size_t)(AS3 char*)smem;
#pragma unroll
        for (int ks = 0; ks < 4; ++ks) {
          const int r0 = ks * 32 + 8 * g + q4;
          u32x2 pa[3][2], vb[2][2];
#pragma unroll
          for (int t = 0; t < 2; ++t) {
#pragma unroll
            for (int i = 0; i < 3; ++i) rdtr(pa[i][t], sb + 75776 + ((hl * 128 + r0 + 4 * t) * 48 + i * 16 + 4 * p4) * 2, 0);
#pragma unroll
            for (int j = 0; j < 2; ++j) rdtr(vb[j][t], sb + 75776 + 49152 + ((hl * 128 + r0 + 4 * t) * 32 + j * 16 + 4 * p4) * 2, 0);
          }
          asm volatile("s_waitcnt lgkmcnt(0)" ::: "memory");
#pragma unroll
          for (int i = 0; i < 3; ++i) { asm volatile("" : "+v"(pa[i][0]), "+v"(pa[i][1])); }
#pragma unroll
          for (int j = 0; j < 2; ++j) { asm volatile("" : "+v"(vb[j][0]), "+v"(vb[j][1])); }
#pragma unroll
          for (int i = 0; i < 3; ++i)
#pragma unroll
            for (int j = 0; j < 2; ++j) {
              u32x4 aw = {pa[i][0][0], pa[i][0][1], pa[i][1][0], pa[i][1][1]};
              u32x4 bw = {vb[j][0][0], vb[j][0][1], vb[j][1][0], vb[j][1][1]};
              a2[i][j] = __builtin_amdgcn_mfma_f32_16x16x32_bf16(__builtin_bit_cast(bf16x8, aw), __builtin_bit_cast(bf16x8, bw), a2[i][j], 0, 0, 0);
            }
        }
        float* kvp = kvpart + ((size_t)tm * H + tn * 8 + hh * 4 + hl) * (48 * 32);
#pragma unroll
        for (int i = 0; i < 3; ++i)
#pragma unroll
          for (int j = 0; j < 2; ++j)
#pragma unroll
            for (int r = 0; r < 4; ++r) kvp[(i * 16 + 4 * g + r) * 32 + j * 16 + fr] = a2[i][j][r];
      }
    }
  }
}

__device__ __forceinline__ void o_step(const u16* __restrict__ qbuf, const u16* __restrict__ wA, const float* __restrict__ kvpart, u16* __restrict__ o,
                                       int h, int rq, char* smem) {
  const int tid = threadIdx.x, lane = tid & 63, wid = tid >> 6, fr = lane & 15, g = lane >> 4;
  float* kvs = (float*)smem;
  for (int i = tid; i < 48 * 32; i += 512) {
    float a = 0.f;
#pragma unroll
    for (int t = 0; t < 16; ++t) a += kvpart[((size_t)t * H + h) * (48 * 32) + i];
    kvs[i] = bfr(a);
  }
  __syncthreads();
  u32x4 ka[2][2];
#pragma unroll
  for (int j = 0; j < 2; ++j) {
    const int d = fr + 16 * j;
#pragma unroll
    for (int e = 0; e < 4; ++e) {
      ka[0][j][e] = pack2(kvs[(e < 2 ? 4 * g + 2 * e : 16 + 4 * g + 2 * (e - 2)) * 32 + d], kvs[(e < 2 ? 4 * g + 2 * e + 1 : 16 + 4 * g + 2 * (e - 2) + 1) * 32 + d]);
      ka[1][j][e] = (g == 0 && e < 2) ? pack2(kvs[(32 + 2 * e) * 32 + d], kvs[(33 + 2 * e) * 32 + d]) : 0u;
    }
  }
  const u16* wa = wA + ((size_t)h * 2 * 64 + lane) * 8;
  const u32x4 wa0 = *(const u32x4*)wa, wa1 = *(const u32x4*)(wa + 64 * 8);
#pragma unroll
  for (int u = 0; u < 4; ++u) {
    const int row = rq * 512 + wid * 64 + u * 16 + fr;
    const unsigned* qr = (const unsigned*)(qbuf + (size_t)row * D + h * DH + 8 * g);
    u32x4 xb;
    if (h == H - 1 && g >= 2) xb = (g == 2) ? (u32x4){qr[0], 0u, 0u, 0u} : (u32x4){0u, 0u, 0u, 0u};
    else xb = (u32x4){qr[0], qr[1], qr[2], qr[3]};
    float e[8], ex[4];
    feat_mfma(xb, wa0, wa1, g == 0, e, ex);
    const u32x4 pb0 = {pack2(e[0], e[1]), pack2(e[2], e[3]), pack2(e[4], e[5]), pack2(e[6], e[7])};
    const u32x4 pb1 = {pack2(ex[0], ex[1]), pack2(ex[2], ex[3]), 0u, 0u};
    f32x4 n[2];
#pragma unroll
    for (int j = 0; j < 2; ++j) {
      f32x4 z = {0.f, 0.f, 0.f, 0.f};
      z = __builtin_amdgcn_mfma_f32_16x16x32_bf16(__builtin_bit_cast(bf16x8, ka[0][j]), __builtin_bit_cast(bf16x8, pb0), z, 0, 0, 0);
      n[j] = __builtin_amdgcn_mfma_f32_16x16x32_bf16(__builtin_bit_cast(bf16x8, ka[1][j]), __builtin_bit_cast(bf16x8, pb1), z, 0, 0, 0);
    }
    float den = g == 0 ? n[1][2] : 0.f;
    den = red16_32_add(den);
    den = bfr(bfr(den) + 1e-6f);
    const float rden = 1.f / den;
    unsigned* orow = (unsigned*)(o + (size_t)row * D + h * DH);
    orow[2 * g] = pack2(bfr(n[0][0]) * rden, bfr(n[0][1]) * rden);
    orow[2 * g + 1] = pack2(bfr(n[0][2]) * rden, bfr(n[0][3]) * rden);
    if (g == 0) orow[8] = pack2(bfr(n[1][0]) * rden, bfr(n[1][1]) * rden);
  }
}

__global__ __launch_bounds__(512) void gemm2_kernel(const u16* __restrict__ h, const u16* __restrict__ w2, float* __restrict__ part,
                                                    const u16* __restrict__ qbuf, const u16* __restrict__ wA, const float* __restrict__ kvpart,
                                                    u16* __restrict__ o) {
  extern __shared__ __attribute__((aligned(16))) char smem[];
  const int tid = threadIdx.x, lane = tid & 63, wid = tid >> 6;
  if (blockIdx.x >= 192) {
    const int b = blockIdx.x - 192;
    o_step(qbuf, wA, kvpart, o, b >> 2, b & 3, smem);
    return;
  }
  const int fr = lane & 15, g = lane >> 4;
  const int split = blockIdx.x & 3, tile = blockIdx.x >> 2;
  const int tm = tile / 3, tn = tile % 3;
  const int row0 = tm * 128, col0 = tn * 192, k0 = split * 1152;
  f32x4 acc[2][6];
  gemm_core<4, 2, 2, 6, 3, 18, 0>(h + (size_t)row0 * F + k0, F, w2 + (size_t)k0 * D + col0, D, smem, acc);
  const int wm = wid >> 1, wn = wid & 1;
  float* P = part + (size_t)split * S * D;
#pragma unroll
  for (int mb = 0; mb < 2; ++mb) {
    const int m = row0 + wm * 32 + mb * 16 + fr;
#pragma unroll
    for (int nb = 0; nb < 6; ++nb) {
      const int n = col0 + wn * 96 + nb * 16 + 4 * g;
      *(f32x4*)(P + (size_t)m * D + n) = acc[mb][nb];
    }
  }
}

__global__ __launch_bounds__(256) void final_kernel(const u16* __restrict__ x, const u16* __restrict__ o, const u16* __restrict__ wo,
                                                    const float* __restrict__ part, u16* __restrict__ out, int nsplit) {
  extern __shared__ __attribute__((aligned(16))) char smem[];
  const int tid = threadIdx.x, lane = tid & 63, wid = tid >> 6;
  const int fr = lane & 15, g = lane >> 4;
  const int tm = blockIdx.x / 9, tn = blockIdx.x % 9;
  const int row0 = tm * 64, col0 = tn * 64;
  const int wm = wid >> 1, wn = wid & 1;
  float res[2][2][4];
#pragma unroll
  for (int mb = 0; mb < 2; ++mb)
#pragma unroll
    for (int nb = 0; nb < 2; ++nb) {
      const int m = row0 + wm * 32 + mb * 16 + fr, n = col0 + wn * 32 + nb * 16 + 4 * g;
      f32x4 a = {0.f, 0.f, 0.f, 0.f};
      for (int sp = 0; sp < nsplit; ++sp) { f32x4 p = *(const f32x4*)(part + (size_t)sp * S * D + (size_t)m * D + n); a += p; }
      for (int r = 0; r < 4; ++r) res[mb][nb][r] = a[r];
    }
#pragma unroll
  for (int mb = 0; mb < 2; ++mb)
#pragma unroll
    for (int nb = 0; nb < 2; ++nb)
      for (int r = 0; r < 4; ++r) asm volatile("" : "+v"(res[mb][nb][r]));
  f32x4 acc[2][2];
  gemm_core<2, 2, 2, 2, 1, 9, 0>(o + (size_t)row0 * D, D, wo + col0, D, smem, acc);
#pragma unroll
  for (int mb = 0; mb < 2; ++mb)
#pragma unroll
    for (int nb = 0; nb < 2; ++nb) {
      const int m = row0 + wm * 32 + mb * 16 + fr, n = col0 + wn * 32 + nb * 16 + 4 * g;
      u32x2 xv = *(const u32x2*)(x + (size_t)m * D + n);
      float xs[4] = {__uint_as_float(xv[0] << 16), __uint_as_float(xv[0] & 0xffff0000u), __uint_as_float(xv[1] << 16), __uint_as_float(xv[1] & 0xffff0000u)};
      float v[4];
      for (int r = 0; r < 4; ++r) v[r] = bfr(xs[r] + bfr(acc[mb][nb][r])) + bfr(res[mb][nb][r]);
      u32x2 w = {pack2(v[0], v[1]), pack2(v[2], v[3])};
      *(u32x2*)(out + (size_t)m * D + n) = w;
    }
}

extern "C" void kernel_launch(void* const* d_in, const int* in_sizes, int n_in, void* d_out, int out_size, void* d_ws, size_t ws_size,
                              hipStream_t stream) {
  const u16* x = (const u16*)d_in[0];
  const u16* wqkv = (const u16*)d_in[1];
  const u16* wphi = (const u16*)d_in[2];
  const u16* wo = (const u16*)d_in[3];
  const u16* w1 = (const u16*)d_in[5];
  const u16* w2 = (const u16*)d_in[6];
  u16* out = (u16*)d_out;
  char* ws = (char*)d_ws;
  size_t off = 0;
  auto take = [&](size_t bytes) { char* p = ws + off; off += (bytes + 255) & ~(size_t)255; return p; };
  u16* xn = (u16*)take((size_t)S * D * 2);
  u16* qbuf = (u16*)take((size_t)S * D * 2);
  u16* h = (u16*)take((size_t)S * F * 2);
  u16* o = (u16*)take((size_t)S * D * 2);
  float* kvpart = (float*)take((size_t)16 * H * 48 * 32 * 4);
  u16* wA = (u16*)take((size_t)H * 2 * 64 * 8 * 2);
  constexpr int NSPLIT = 4;
  float* part = (float*)take((size_t)NSPLIT * S * D * 4);

  ln_kernel<<<S / 4, 256, 0, stream>>>(x, xn, wphi, wA);
  const int shm1 = 160 * 1024, shm2 = (3 * 128 + 2 * 192) * 128;
  hipFuncSetAttribute((const void*)gemm1_kernel, hipFuncAttributeMaxDynamicSharedMemorySize, shm1);
  hipFuncSetAttribute((const void*)gemm2_kernel, hipFuncAttributeMaxDynamicSharedMemorySize, shm2);
  gemm1_kernel<<<256, 512, shm1, stream>>>(xn, w1, wqkv, wA, h, qbuf, kvpart);
  gemm2_kernel<<<192 + 128, 512, shm2, stream>>>(h, w2, part, qbuf, wA, kvpart, o);
  final_kernel<<<32 * 9, 256, (3 * 64 + 2 * 64) * 128, stream>>>(x, o, wo, part, out, NSPLIT);
}
```

```cpp
#include <hip/hip_runtime.h>
#include <hip/hip_bf16.h>

typedef unsigned short u16;
typedef __attribute__((ext_vector_type(8))) __bf16 bf16x8;
typedef __attribute__((ext_vector_type(4))) float f32x4;
typedef __attribute__((ext_vector_type(4))) unsigned u32x4;
typedef __attribute__((ext_vector_type(2))) unsigned u32x2;
#define AS1 __attribute__((address_space(1)))
#define AS3 __attribute__((address_space(3)))
typedef __attribute__((ext_vector_type(2))) float f32x2;
typedef const __attribute__((address_space(4))) float* cfp;

constexpr int S = 2048, D = 576, H = 32, DH = 18, F = 4608;
constexpr int NQKV = 3 * D;
constexpr int NFEAT = 2 * DH;
constexpr int KVSZ = NFEAT * DH + NFEAT;

__device__ __forceinline__ float bf2f(u16 v) { return __uint_as_float(((unsigned)v) << 16); }
__device__ __forceinline__ u16 f2bf(float f) { __bf16 b = (__bf16)f; return __builtin_bit_cast(u16, b); }
__device__ __forceinline__ unsigned pack2(float lo, float hi) { return (unsigned)f2bf(lo) | ((unsigned)f2bf(hi) << 16); }
__device__ __forceinline__ float bfr(float f) { return bf2f(f2bf(f)); }
__device__ __forceinline__ float silu_f(float v) { return v * __builtin_amdgcn_rcpf(1.f + __builtin_amdgcn_exp2f(-1.4426950408889634f * v)); }

#define SBAR() __builtin_amdgcn_sched_barrier(0)
template <int N> __device__ __forceinline__ void waitv() { asm volatile("s_waitcnt vmcnt(%0)" ::"n"(N) : "memory"); }
__device__ __forceinline__ void rd128(u32x4& d, unsigned addr, int off) { asm volatile("ds_read_b128 %0, %1 offset:%2" : "=v"(d) : "v"(addr), "i"(off)); }
__device__ __forceinline__ void rdtr(u32x2& d, unsigned addr, int off) { asm volatile("ds_read_b64_tr_b16 %0, %1 offset:%2" : "=v"(d) : "v"(addr), "i"(off)); }

typedef __attribute__((ext_vector_type(2))) __bf16 bf16x2;
__device__ __forceinline__ float dot2(unsigned a, unsigned b, float c) { return __builtin_amdgcn_fdot2_f32_bf16(__builtin_bit_cast(bf16x2, a), __builtin_bit_cast(bf16x2, b), c, false); }
__device__ __forceinline__ void load_wa(const u16* __restrict__ wphi, int head, int lane, u32x4& wa0, u32x4& wa1) {
  const int fr = lane & 15, g = lane >> 4;
  const u16* w = wphi + head * (DH * DH) + fr;
  unsigned a[8], b[8];
#pragma unroll
  for (int j = 0; j < 8; ++j) {
    const int d = 8 * g + j;
    a[j] = d < DH ? w[d * DH] : 0u;
    b[j] = (d < DH && fr < 2) ? w[d * DH + 16] : 0u;
  }
#pragma unroll
  for (int e = 0; e < 4; ++e) { wa0[e] = a[2 * e] | (a[2 * e + 1] << 16); wa1[e] = b[2 * e] | (b[2 * e + 1] << 16); }
}

__device__ __forceinline__ float red16_32_max(float v) {
  auto r = __builtin_amdgcn_permlane16_swap(__float_as_uint(v), __float_as_uint(v), false, false);
  v = fmaxf(__uint_as_float(r[0]), __uint_as_float(r[1]));
  r = __builtin_amdgcn_permlane32_swap(__float_as_uint(v), __float_as_uint(v), false, false);
  return fmaxf(__uint_as_float(r[0]), __uint_as_float(r[1]));
}
__device__ __forceinline__ float red16_32_add(float v) {
  auto r = __builtin_amdgcn_permlane16_swap(__float_as_uint(v), __float_as_uint(v), false, false);
  v = __uint_as_float(r[0]) + __uint_as_float(r[1]);
  r = __builtin_amdgcn_permlane32_swap(__float_as_uint(v), __float_as_uint(v), false, false);
  return __uint_as_float(r[0]) + __uint_as_float(r[1]);
}
template <int WM, int WN, int MB, int NB, int BSUB, int NK, int QKVMAP, int LNF = 0>
__device__ __forceinline__ void gemm_core(const u16* __restrict__ A, int lda, const u16* __restrict__ B, int ldb, char* smem, f32x4 (&acc)[MB][NB]) {
  constexpr int BM = WM * MB * 16, NW = WM * WN;
#define CBMAP(wn_, nb_) (QKVMAP ? (nb_) * WN + (wn_) : (wn_) * NB + (nb_))
  constexpr int ABYTES = BM * 128, BBYTES = BSUB * 8192;
  constexpr int BOFF = 3 * ABYTES;
  constexpr int APW = BM / 8 / NW, BPW = BSUB * 8 / NW;
  static_assert((BM / 8) % NW == 0 && (BSUB * 8) % NW == 0, "pieces");
  int tid = threadIdx.x; asm volatile("" : "+v"(tid));
  const int lane = tid & 63, wid = __builtin_amdgcn_readfirstlane(tid >> 6);
  const int wm = wid / WN, wn = wid % WN;
  unsigned aoffs[APW], boffs[BPW];
  {
    const int r8 = lane >> 3, c = lane & 7;
#pragma unroll
    for (int p = 0; p < APW; ++p) {
      const int piece = p * NW + wid;
      aoffs[p] = (unsigned)((piece * 8 + r8) * lda + ((c ^ r8) << 3)) * 2u;
    }
#pragma unroll
    for (int p = 0; p < BPW; ++p) {
      const int pb = p * NW + wid, sub = pb >> 3, j = pb & 7;
      const int r = j * 8 + r8;
      const int swz = (((r >> 1) & 1) << 1) | ((r >> 3) & 1);
      const int chunk = (((c >> 1) ^ swz) << 1) | (c & 1);
      int col = sub * 64 + (chunk << 3);
      if (QKVMAP) {
        const int c8 = chunk << 3;
        col = c8 + (p == 2 && c8 >= 16 ? 432 : 0) + (p == 4 && c8 >= 32 ? 432 : 0) - (p == 6 && c8 >= 48 ? 16 : 0);
      }
      boffs[p] = (unsigned)(r * ldb + col) * 2u;
    }
  }
  auto stageA1 = [&](int slot, int p, int kt) {
    __builtin_amdgcn_global_load_lds((const AS1 void*)((const char*)(A + kt * 64 + (size_t)p * NW * 8 * lda) + aoffs[0]), (AS3 void*)(smem + slot * ABYTES + (p * NW + wid) * 1024), 16, 0, 0);
  };
  auto stageB1 = [&](int slot, int p, int kt) {
    __builtin_amdgcn_global_load_lds((const AS1 void*)((const char*)(B + (size_t)kt * 64 * ldb + (QKVMAP ? p * 64 + (p > 4 ? 864 : p > 2 ? 432 : 0) : ((p * NW) >> 3) * 64 + ((p * NW) & 7) * 8 * ldb)) + (QKVMAP ? boffs[p & 1 ? 0 : p] : boffs[0])), (AS3 void*)(smem + BOFF + slot * BBYTES + (p * NW + wid) * 1024), 16, 0, 0);
  };
#pragma unroll
  for (int p = 0; p < BPW; ++p) stageB1(0, p, 0);
#pragma unroll
  for (int p = 0; p < APW; ++p) stageA1(0, p, 0);
#pragma unroll
  for (int p = 0; p < APW; ++p) stageA1(1, p, 1);

  const int fr = lane & 15, g = lane >> 4, q = (lane & 15) >> 2, p4 = lane & 3;
  const unsigned sbase = (unsigned)(size_t)(AS3 char*)smem;
  constexpr bool AIMM = (2 * ABYTES + (MB - 1) * 2048) < 65536;
  unsigned aaddr[2][AIMM ? 1 : 3];
  {
    const int r = wm * MB * 16 + fr;
#pragma unroll
    for (int s = 0; s < (AIMM ? 1 : 3); ++s) {
      aaddr[0][s] = sbase + s * ABYTES + r * 128 + (((0 + g) ^ (r & 7)) << 4);
      aaddr[1][s] = sbase + s * ABYTES + r * 128 + (((4 + g) ^ (r & 7)) << 4);
    }
  }
  unsigned baddr[NB];
  {
    const int sB = (((q >> 1) & 1) << 1) | (g & 1);
    const int kr = 8 * g + q;
#pragma unroll
    for (int nb = 0; nb < NB; ++nb) {
      const int cb = CBMAP(wn, nb);
      baddr[nb] = sbase + BOFF + (QKVMAP ? nb * BBYTES : (cb >> 2) * 8192) + kr * 128 + ((((cb & 3) ^ sB)) << 5) + ((p4 >> 1) << 4) + 8 * (p4 & 1);
    }
  }
#pragma unroll
  for (int i = 0; i < MB; ++i)
#pragma unroll
    for (int j = 0; j < NB; ++j) acc[i][j] = (f32x4){0.f, 0.f, 0.f, 0.f};
  u32x4 af[2][MB];
  u32x2 bt[2][NB][2];
  constexpr int RPW = MB / WN > 0 ? MB / WN : 1, NBG = (NB + WM - 1) / WM;
  float rsm[RPW], rq[RPW], cs_[NBG];
#pragma unroll
  for (int i = 0; i < RPW; ++i) rsm[i] = rq[i] = 0.f;
#pragma unroll
  for (int j = 0; j < NBG; ++j) cs_[j] = 0.f;
  auto wait_set = [&](int set) {
#pragma unroll
    for (int i = 0; i < MB; ++i) asm volatile("" : "+v"(af[set][i]));
    asm volatile("s_waitcnt lgkmcnt(0)" ::: "memory");
#pragma unroll
    for (int i = 0; i < MB; ++i) asm volatile("" : "+v"(af[set][i]));
#pragma unroll
    for (int j = 0; j < NB; ++j) asm volatile("" : "+v"(bt[set][j][0]), "+v"(bt[set][j][1]));
  };
  constexpr int NRD = 2 * NB + MB;
  constexpr int NRS = NRD < MB * NB ? NRD : MB * NB;
#define PHASE(cs, rs, aslot, bslot, kk, do_reads, sb, sa, kb, ka)                                                                            \
  _Pragma("unroll") for (int i = 0; i < MB * NB; ++i) {                                                                              \
    const int mb = i / NB, nb = i % NB;                                                                                              \
    u32x4 bw = {bt[cs][nb][0][0], bt[cs][nb][0][1], bt[cs][nb][1][0], bt[cs][nb][1][1]};                                            \
    acc[mb][nb] = __builtin_amdgcn_mfma_f32_16x16x32_bf16(__builtin_bit_cast(bf16x8, bw), __builtin_bit_cast(bf16x8, af[cs][mb]), acc[mb][nb], 0, 0, 0); \
    SBAR();                                                                                                                          \
    if (LNF && i == 1) {                                                                                                             \
      _Pragma("unroll") for (int ii = 0; ii < RPW; ++ii) {                                                                           \
        u32x4 f = af[cs][ii];                                                                                                        \
        _Pragma("unroll") for (int w = 1; w < WN; ++w) f = (wn == w) ? af[cs][w * RPW + ii] : f;                                     \
        _Pragma("unroll") for (int e = 0; e < 4; ++e) { rsm[ii] = dot2(f[e], 0x3f803f80u, rsm[ii]); rq[ii] = dot2(f[e], f[e], rq[ii]); } \
      }                                                                                                                              \
      _Pragma("unroll") for (int jj = 0; jj < NBG; ++jj) {                                                                           \
        u32x2 f0 = bt[cs][jj][0], f1 = bt[cs][jj][1];                                                                                \
        _Pragma("unroll") for (int w = 1; w < WM; ++w) { const int nbw = w * NBG + jj < NB ? w * NBG + jj : jj;                      \
          f0 = (wm == w) ? bt[cs][nbw][0] : f0; f1 = (wm == w) ? bt[cs][nbw][1] : f1; }                                              \
        cs_[jj] = dot2(f0[0], 0x3f803f80u, dot2(f0[1], 0x3f803f80u, dot2(f1[0], 0x3f803f80u, dot2(f1[1], 0x3f803f80u, cs_[jj]))));  \
      }                                                                                                                              \
      SBAR();                                                                                                                        \
    }                                                                                                                                \
    if (do_reads) {                                                                                                                  \
      _Pragma("unroll") for (int r = (i < NRS ? (i * NRD) / NRS : NRD); r < (i < NRS ? ((i + 1) * NRD) / NRS : NRD); ++r) {          \
        if (r < 2 * NB) rdtr(bt[rs][r >> 1][r & 1], QKVMAP ? baddr[bslot] : baddr[r >> 1], (QKVMAP ? (r >> 1) * 8192 : (bslot) * BBYTES) + (kk) * 4096 + (r & 1) * 512);                  \
        else rd128(af[rs][r - 2 * NB], aaddr[kk][AIMM ? 0 : (aslot)], (AIMM ? (aslot) * ABYTES : 0) + (r - 2 * NB) * 2048);                                                       \
        SBAR();                                                                                                                      \
      }                                                                                                                              \
    }                                                                                                                                \
    _Pragma("unroll") for (int s = (i * (APW + BPW)) / (MB * NB); s < ((i + 1) * (APW + BPW)) / (MB * NB); ++s) {                  \
      if (s < BPW) { if ((sb) >= 0) stageB1(sb, s, kb); }                                                                                 \
      else { if ((sa) >= 0) stageA1(sa, s - BPW, ka); }                                                                                 \
      SBAR();                                                                                                                        \
    }                                                                                                                                \
  }
  waitv<APW>();
  __builtin_amdgcn_s_barrier();
#pragma unroll
  for (int nb = 0; nb < NB; ++nb) { rdtr(bt[0][nb][0], baddr[QKVMAP ? 0 : nb], QKVMAP ? nb * 8192 : 0); rdtr(bt[0][nb][1], baddr[QKVMAP ? 0 : nb], (QKVMAP ? nb * 8192 : 0) + 512); }
#pragma unroll
  for (int mb = 0; mb < MB; ++mb) rd128(af[0][mb], aaddr[0][0], mb * 2048);
#pragma unroll
  for (int it = 0; it < NK; ++it) {
    wait_set(0);
    SBAR();
    PHASE(0, 1, it % 3, it & 1, 1, true, it == 0 ? 1 : -1, it == 0 ? 2 : -1, 1, 2);
    wait_set(1);
    if (it + 2 < NK) waitv<APW>(); else waitv<0>();
    __builtin_amdgcn_s_barrier();
    SBAR();
    PHASE(1, 0, (it + 1) % 3, (it + 1) & 1, 0, it + 1 < NK, (it + 2 < NK) ? (it & 1) : -1, (it + 3 < NK) ? (it % 3) : -1, it + 2, it + 3);
  }
#undef PHASE
  if (LNF) {
    constexpr int BN = WN * NB * 16;
    float* st = (float*)smem;
#pragma unroll
    for (int i = 0; i < RPW; ++i) { rsm[i] = red16_32_add(rsm[i]); rq[i] = red16_32_add(rq[i]); }
#pragma unroll
    for (int j = 0; j < NBG; ++j) cs_[j] = red16_32_add(cs_[j]);
    __syncthreads();
    if (g == 0) {
#pragma unroll
      for (int i = 0; i < RPW; ++i) { const int r = (wm * MB + wn * RPW + i) * 16 + fr; st[r] = rsm[i]; st[BM + r] = rq[i]; }
#pragma unroll
      for (int j = 0; j < NBG; ++j) if (wm * NBG + j < NB) st[2 * BM + CBMAP(wn, wm * NBG + j) * 16 + fr] = cs_[j];
    }
    __syncthreads();
    float mean[MB], rstd[MB];
#pragma unroll
    for (int mb = 0; mb < MB; ++mb) {
      const int r = (wm * MB + mb) * 16 + fr;
      mean[mb] = st[r] * (1.f / D);
      rstd[mb] = rsqrtf(fmaxf(st[BM + r] * (1.f / D) - mean[mb] * mean[mb], 0.f) + 1e-5f);
    }
#pragma unroll
    for (int nb = 0; nb < NB; ++nb) {
      const f32x4 c4 = *(const f32x4*)(st + 2 * BM + CBMAP(wn, nb) * 16 + 4 * g);
#pragma unroll
      for (int mb = 0; mb < MB; ++mb)
#pragma unroll
        for (int e = 0; e < 4; ++e) acc[mb][nb][e] = rstd[mb] * (acc[mb][nb][e] - mean[mb] * c4[e]);
    }
  }
}

__device__ __forceinline__ void feat_mfma(u32x4 xb, u32x4 wa0, u32x4 wa1, bool g0, float (&e)[8], float (&ex)[4]) {
  f32x4 z = {0.f, 0.f, 0.f, 0.f};
  f32x4 p0 = __builtin_amdgcn_mfma_f32_16x16x32_bf16(__builtin_bit_cast(bf16x8, wa0), __builtin_bit_cast(bf16x8, xb), z, 0, 0, 0);
  f32x4 p1 = __builtin_amdgcn_mfma_f32_16x16x32_bf16(__builtin_bit_cast(bf16x8, wa1), __builtin_bit_cast(bf16x8, xb), z, 0, 0, 0);
  float p[4], q[2];
#pragma unroll
  for (int r = 0; r < 4; ++r) p[r] = bfr(p0[r]);
  q[0] = bfr(p1[0]); q[1] = bfr(p1[1]);
  float mx = fmaxf(fmaxf(p[0], p[1]), fmaxf(p[2], p[3])), mn = fminf(fminf(p[0], p[1]), fminf(p[2], p[3]));
  if (g0) { mx = fmaxf(mx, fmaxf(q[0], q[1])); mn = fminf(mn, fminf(q[0], q[1])); }
  mx = red16_32_max(mx);
  mn = -red16_32_max(-mn);
  float s1 = 0.f, s2 = 0.f;
#pragma unroll
  for (int r = 0; r < 4; ++r) { e[r] = __expf(p[r] - mx); e[4 + r] = __expf(mn - p[r]); s1 += e[r]; s2 += e[4 + r]; }
#pragma unroll
  for (int r = 0; r < 2; ++r) { ex[r] = g0 ? __expf(q[r] - mx) : 0.f; ex[2 + r] = g0 ? __expf(mn - q[r]) : 0.f; s1 += ex[r]; s2 += ex[2 + r]; }
  s1 = red16_32_add(s1);
  s2 = red16_32_add(s2);
  const float r1 = 1.f / s1, r2 = 1.f / s2;
#pragma unroll
  for (int r = 0; r < 4; ++r) { e[r] = bfr(e[r] * r1); e[4 + r] = bfr(e[4 + r] * r2); }
  ex[0] = bfr(ex[0] * r1); ex[1] = bfr(ex[1] * r1); ex[2] = bfr(ex[2] * r2); ex[3] = bfr(ex[3] * r2);
}

__global__ __launch_bounds__(512) void gemm1_kernel(const u16* __restrict__ xn, const u16* __restrict__ w1, const u16* __restrict__ wqkv,
                                                    const u16* __restrict__ wA, u16* __restrict__ h, u16* __restrict__ qbuf, float* __restrict__ kvpart) {
  extern __shared__ __attribute__((aligned(16))) char smem[];
  if (blockIdx.x < 192) {
    const int tm = blockIdx.x / 24, tn = blockIdx.x % 24;
    const int row0 = tm * 256, col0 = tn * 192;
    f32x4 acc[4][6];
    gemm_core<4, 2, 4, 6, 3, 9, 0, 1>(xn + (size_t)row0 * D, D, w1 + col0, F, smem, acc);
    int tid = threadIdx.x; asm volatile("" : "+v"(tid));
    const int lane = tid & 63, wid = tid >> 6, fr = lane & 15, g = lane >> 4;
    const int wm = wid >> 1, wn = wid & 1;
#pragma unroll
    for (int mb = 0; mb < 4; ++mb) {
      const int m = row0 + wm * 64 + mb * 16 + fr;
#pragma unroll
      for (int nb = 0; nb < 6; ++nb) {
        const int n = col0 + wn * 96 + nb * 16 + 4 * g;
        f32x4 v = acc[mb][nb];
        for (int r = 0; r < 4; ++r) v[r] = silu_f(bfr(v[r]));
        u32x2 w = {pack2(v[0], v[1]), pack2(v[2], v[3])};
        *(u32x2*)(h + (size_t)m * F + n) = w;
      }
    }
  } else {
    const int b = blockIdx.x - 192;
    const int tm = b >> 2, tn = b & 3;
    const int row0 = tm * 128;
    f32x4 acc[4][7];
    gemm_core<2, 4, 4, 7, 7, 9, 1, 1>(xn + (size_t)row0 * D, D, wqkv + tn * 144, NQKV, smem, acc);
    int tid = threadIdx.x; asm volatile("" : "+v"(tid));
    const int lane = tid & 63, wid = tid >> 6, fr = lane & 15, g = lane >> 4;
    const int wm = wid >> 2, wn = wid & 3;
    constexpr int TP = 296;
    u16* T = (u16*)smem;
    u16* PH = (u16*)(smem + 75776);
    u16* VV = (u16*)(smem + 75776 + 49152);
    __syncthreads();
#pragma unroll
    for (int mb = 0; mb < 4; ++mb) {
      const int ml = wm * 64 + mb * 16 + fr;
#pragma unroll
      for (int nb = 0; nb < 7; ++nb) {
        const int cb = nb * 4 + wn;
        if (cb < 27) {
          f32x4 v = acc[mb][nb];
          u32x2 w = {pack2(v[0], v[1]), pack2(v[2], v[3])};
          if (cb < 9) *(u32x2*)(qbuf + (size_t)(row0 + ml) * D + tn * 144 + cb * 16 + 4 * g) = w;
          else *(u32x2*)(T + ml * TP + (cb - 9) * 16 + 4 * g) = w;
        }
      }
    }
#pragma unroll 1
    for (int hh = 0; hh < 2; ++hh) {
      __syncthreads();
#pragma unroll
      for (int u = 0; u < 4; ++u) {
        const int unit = wid * 4 + u, hl = unit >> 3, rb = unit & 7;
        const int head = hh * 4 + hl;
        u32x4 wa0, wa1;
        load_wa(wA, tn * 8 + head, lane, wa0, wa1);
        const unsigned* kr = (const unsigned*)(T + (rb * 16 + fr) * TP + head * DH + 8 * g);
        const u32x4 xb = {kr[0], kr[1], kr[2], kr[3]};
        float e[8], ex[4];
        feat_mfma(xb, wa0, wa1, g == 0, e, ex);
        u16* pr = PH + (hl * 128 + rb * 16 + fr) * 48;
        *(u32x2*)(pr + 4 * g) = (u32x2){pack2(e[0], e[1]), pack2(e[2], e[3])};
        *(u32x2*)(pr + 16 + 4 * g) = (u32x2){pack2(e[4], e[5]), pack2(e[6], e[7])};
        if (g == 0) *(u32x2*)(pr + 32) = (u32x2){pack2(ex[0], ex[1]), pack2(ex[2], ex[3])};
        else *(u32x2*)(pr + 32 + 4 * g) = (u32x2){0u, 0u};
        const unsigned* vr = (const unsigned*)(T + (rb * 16 + fr) * TP + 144 + head * DH);
        u32x4 vv;
        if (g < 2) vv = (u32x4){vr[4 * g], vr[4 * g + 1], vr[4 * g + 2], vr[4 * g + 3]};
        else if (g == 2) vv = (u32x4){vr[8], 0x3f80u, 0u, 0u};
        else vv = (u32x4){0u, 0u, 0u, 0u};
        *(u32x4*)(VV + (hl * 128 + rb * 16 + fr) * 32 + 8 * g) = vv;
      }
      __syncthreads();
      if (wid < 4) {
        const int hl = wid;
        f32x4 a2[3][2];
#pragma unroll
        for (int i = 0; i < 3; ++i)
#pragma unroll
          for (int j = 0; j < 2; ++j) a2[i][j] = (f32x4){0.f, 0.f, 0.f, 0.f};
        const int q4 = (lane & 15) >> 2, p4 = lane & 3;
        const unsigned sb = (unsigned)(size_t)(AS3 char*)smem;
#pragma unroll
        for (int ks = 0; ks < 4; ++ks) {
          const int r0 = ks * 32 + 8 * g + q4;
          u32x2 pa[3][2], vb[2][2];
#pragma unroll
          for (int t = 0; t < 2; ++t) {
#pragma unroll
            for (int i = 0; i < 3; ++i) rdtr(pa[i][t], sb + 75776 + ((hl * 128 + r0 + 4 * t) * 48 + i * 16 + 4 * p4) * 2, 0);
#pragma unroll
            for (int j = 0; j < 2; ++j) rdtr(vb[j][t], sb + 75776 + 49152 + ((hl * 128 + r0 + 4 * t) * 32 + j * 16 + 4 * p4) * 2, 0);
          }
          asm volatile("s_waitcnt lgkmcnt(0)" ::: "memory");
#pragma unroll
          for (int i = 0; i < 3; ++i) { asm volatile("" : "+v"(pa[i][0]), "+v"(pa[i][1])); }
#pragma unroll
          for (int j = 0; j < 2; ++j) { asm volatile("" : "+v"(vb[j][0]), "+v"(vb[j][1])); }
#pragma unroll
          for (int i = 0; i < 3; ++i)
#pragma unroll
            for (int j = 0; j < 2; ++j) {
              u32x4 aw = {pa[i][0][0], pa[i][0][1], pa[i][1][0], pa[i][1][1]};
              u32x4 bw = {vb[j][0][0], vb[j][0][1], vb[j][1][0], vb[j][1][1]};
              a2[i][j] = __builtin_amdgcn_mfma_f32_16x16x32_bf16(__builtin_bit_cast(bf16x8, aw), __builtin_bit_cast(bf16x8, bw), a2[i][j], 0, 0, 0);
            }
        }
        float* kvp = kvpart + ((size_t)tm * H + tn * 8 + hh * 4 + hl) * (48 * 32);
#pragma unroll
        for (int i = 0; i < 3; ++i)
#pragma unroll
          for (int j = 0; j < 2; ++j)
#pragma unroll
            for (int r = 0; r < 4; ++r) kvp[(i * 16 + 4 * g + r) * 32 + j * 16 + fr] = a2[i][j][r];
      }
    }
  }
}

__device__ __forceinline__ void o_step(const u16* __restrict__ qbuf, const u16* __restrict__ wA, const float* __restrict__ kvpart, u16* __restrict__ o,
                                       int h, int rq, char* smem) {
  const int tid = threadIdx.x, lane = tid & 63, wid = tid >> 6, fr = lane & 15, g = lane >> 4;
  float* kvs = (float*)smem;
  for (int i = tid; i < 48 * 32; i += 512) {
    float a = 0.f;
#pragma unroll
    for (int t = 0; t < 16; ++t) a += kvpart[((size_t)t * H + h) * (48 * 32) + i];
    kvs[i] = bfr(a);
  }
  __syncthreads();
  u32x4 ka[2][2];
#pragma unroll
  for (int j = 0; j < 2; ++j) {
    const int d = fr + 16 * j;
#pragma unroll
    for (int e = 0; e < 4; ++e) {
      ka[0][j][e] = pack2(kvs[(e < 2 ? 4 * g + 2 * e : 16 + 4 * g + 2 * (e - 2)) * 32 + d], kvs[(e < 2 ? 4 * g + 2 * e + 1 : 16 + 4 * g + 2 * (e - 2) + 1) * 32 + d]);
      ka[1][j][e] = (g == 0 && e < 2) ? pack2(kvs[(32 + 2 * e) * 32 + d], kvs[(33 + 2 * e) * 32 + d]) : 0u;
    }
  }
  u32x4 wa0, wa1;
  load_wa(wA, h, lane, wa0, wa1);
#pragma unroll
  for (int u = 0; u < 4; ++u) {
    const int row = rq * 512 + wid * 64 + u * 16 + fr;
    const unsigned* qr = (const unsigned*)(qbuf + (size_t)row * D + h * DH + 8 * g);
    u32x4 xb;
    if (h == H - 1 && g >= 2) xb = (g == 2) ? (u32x4){qr[0], 0u, 0u, 0u} : (u32x4){0u, 0u, 0u, 0u};
    else xb = (u32x4){qr[0], qr[1], qr[2], qr[3]};
    float e[8], ex[4];
    feat_mfma(xb, wa0, wa1, g == 0, e, ex);
    const u32x4 pb0 = {pack2(e[0], e[1]), pack2(e[2], e[3]), pack2(e[4], e[5]), pack2(e[6], e[7])};
    const u32x4 pb1 = {pack2(ex[0], ex[1]), pack2(ex[2], ex[3]), 0u, 0u};
    f32x4 n[2];
#pragma unroll
    for (int j = 0; j < 2; ++j) {
      f32x4 z = {0.f, 0.f, 0.f, 0.f};
      z = __builtin_amdgcn_mfma_f32_16x16x32_bf16(__builtin_bit_cast(bf16x8, ka[0][j]), __builtin_bit_cast(bf16x8, pb0), z, 0, 0, 0);
      n[j] = __builtin_amdgcn_mfma_f32_16x16x32_bf16(__builtin_bit_cast(bf16x8, ka[1][j]), __builtin_bit_cast(bf16x8, pb1), z, 0, 0, 0);
    }
    float den = g == 0 ? n[1][2] : 0.f;
    den = red16_32_add(den);
    den = bfr(bfr(den) + 1e-6f);
    const float rden = 1.f / den;
    unsigned* orow = (unsigned*)(o + (size_t)row * D + h * DH);
    orow[2 * g] = pack2(bfr(n[0][0]) * rden, bfr(n[0][1]) * rden);
    orow[2 * g + 1] = pack2(bfr(n[0][2]) * rden, bfr(n[0][3]) * rden);
    if (g == 0) orow[8] = pack2(bfr(n[1][0]) * rden, bfr(n[1][1]) * rden);
  }
}

__global__ __launch_bounds__(512) void gemm2_kernel(const u16* __restrict__ h, const u16* __restrict__ w2, float* __restrict__ part,
                                                    const u16* __restrict__ qbuf, const u16* __restrict__ wA, const float* __restrict__ kvpart,
                                                    u16* __restrict__ o) {
  extern __shared__ __attribute__((aligned(16))) char smem[];
  const int tid = threadIdx.x, lane = tid & 63, wid = tid >> 6;
  if (blockIdx.x >= 192) {
    const int b = blockIdx.x - 192;
    o_step(qbuf, wA, kvpart, o, b >> 2, b & 3, smem);
    return;
  }
  const int fr = lane & 15, g = lane >> 4;
  const int split = blockIdx.x & 3, tile = blockIdx.x >> 2;
  const int tm = tile / 3, tn = tile % 3;
  const int row0 = tm * 128, col0 = tn * 192, k0 = split * 1152;
  f32x4 acc[2][6];
  gemm_core<4, 2, 2, 6, 3, 18, 0>(h + (size_t)row0 * F + k0, F, w2 + (size_t)k0 * D + col0, D, smem, acc);
  const int wm = wid >> 1, wn = wid & 1;
  float* P = part + (size_t)split * S * D;
#pragma unroll
  for (int mb = 0; mb < 2; ++mb) {
    const int m = row0 + wm * 32 + mb * 16 + fr;
#pragma unroll
    for (int nb = 0; nb < 6; ++nb) {
      const int n = col0 + wn * 96 + nb * 16 + 4 * g;
      *(f32x4*)(P + (size_t)m * D + n) = acc[mb][nb];
    }
  }
}

__global__ __launch_bounds__(256) void final_kernel(const u16* __restrict__ x, const u16* __restrict__ o, const u16* __restrict__ wo,
                                                    const float* __restrict__ part, u16* __restrict__ out, int nsplit) {
  extern __shared__ __attribute__((aligned(16))) char smem[];
  const int tid = threadIdx.x, lane = tid & 63, wid = tid >> 6;
  const int fr = lane & 15, g = lane >> 4;
  const int tm = blockIdx.x / 9, tn = blockIdx.x % 9;
  const int row0 = tm * 64, col0 = tn * 64;
  const int wm = wid >> 1, wn = wid & 1;
  float res[2][2][4];
#pragma unroll
  for (int mb = 0; mb < 2; ++mb)
#pragma unroll
    for (int nb = 0; nb < 2; ++nb) {
      const int m = row0 + wm * 32 + mb * 16 + fr, n = col0 + wn * 32 + nb * 16 + 4 * g;
      f32x4 a = {0.f, 0.f, 0.f, 0.f};
      for (int sp = 0; sp < nsplit; ++sp) { f32x4 p = *(const f32x4*)(part + (size_t)sp * S * D + (size_t)m * D + n); a += p; }
      for (int r = 0; r < 4; ++r) res[mb][nb][r] = a[r];
    }
#pragma unroll
  for (int mb = 0; mb < 2; ++mb)
#pragma unroll
    for (int nb = 0; nb < 2; ++nb)
      for (int r = 0; r < 4; ++r) asm volatile("" : "+v"(res[mb][nb][r]));
  f32x4 acc[2][2];
  gemm_core<2, 2, 2, 2, 1, 9, 0>(o + (size_t)row0 * D, D, wo + col0, D, smem, acc);
#pragma unroll
  for (int mb = 0; mb < 2; ++mb)
#pragma unroll
    for (int nb = 0; nb < 2; ++nb) {
      const int m = row0 + wm * 32 + mb * 16 + fr, n = col0 + wn * 32 + nb * 16 + 4 * g;
      u32x2 xv = *(const u32x2*)(x + (size_t)m * D + n);
      float xs[4] = {__uint_as_float(xv[0] << 16), __uint_as_float(xv[0] & 0xffff0000u), __uint_as_float(xv[1] << 16), __uint_as_float(xv[1] & 0xffff0000u)};
      float v[4];
      for (int r = 0; r < 4; ++r) v[r] = bfr(xs[r] + bfr(acc[mb][nb][r])) + bfr(res[mb][nb][r]);
      u32x2 w = {pack2(v[0], v[1]), pack2(v[2], v[3])};
      *(u32x2*)(out + (size_t)m * D + n) = w;
    }
}

extern "C" void kernel_launch(void* const* d_in, const int* in_sizes, int n_in, void* d_out, int out_size, void* d_ws, size_t ws_size,
                              hipStream_t stream) {
  const u16* x = (const u16*)d_in[0];
  const u16* wqkv = (const u16*)d_in[1];
  const u16* wphi = (const u16*)d_in[2];
  const u16* wo = (const u16*)d_in[3];
  const u16* w1 = (const u16*)d_in[5];
  const u16* w2 = (const u16*)d_in[6];
  u16* out = (u16*)d_out;
  char* ws = (char*)d_ws;
  size_t off = 0;
  auto take = [&](size_t bytes) { char* p = ws + off; off += (bytes + 255) & ~(size_t)255; return p; };
  u16* qbuf = (u16*)take((size_t)S * D * 2);
  u16* h = (u16*)take((size_t)S * F * 2);
  u16* o = (u16*)take((size_t)S * D * 2);
  float* kvpart = (float*)take((size_t)16 * H * 48 * 32 * 4);
  const u16* wA = wphi;
  const u16* xn = x;
  constexpr int NSPLIT = 4;
  float* part = (float*)take((size_t)NSPLIT * S * D * 4);

  const int shm1 = 160 * 1024, shm2 = (3 * 128 + 2 * 192) * 128;
  hipFuncSetAttribute((const void*)gemm1_kernel, hipFuncAttributeMaxDynamicSharedMemorySize, shm1);
  hipFuncSetAttribute((const void*)gemm2_kernel, hipFuncAttributeMaxDynamicSharedMemorySize, shm2);
  gemm1_kernel<<<256, 512, shm1, stream>>>(xn, w1, wqkv, wA, h, qbuf, kvpart);
  gemm2_kernel<<<192 + 128, 512, shm2, stream>>>(h, w2, part, qbuf, wA, kvpart, o);
  final_kernel<<<32 * 9, 256, (3 * 64 + 2 * 64) * 128, stream>>>(x, o, wo, part, out, NSPLIT);
}
```

```cpp
#include <hip/hip_runtime.h>
#include <hip/hip_bf16.h>

typedef unsigned short u16;
typedef __attribute__((ext_vector_type(8))) __bf16 bf16x8;
typedef __attribute__((ext_vector_type(4))) float f32x4;
typedef __attribute__((ext_vector_type(4))) unsigned u32x4;
typedef __attribute__((ext_vector_type(2))) unsigned u32x2;
#define AS1 __attribute__((address_space(1)))
#define AS3 __attribute__((address_space(3)))
typedef __attribute__((ext_vector_type(2))) float f32x2;
typedef const __attribute__((address_space(4))) float* cfp;

constexpr int S = 2048, D = 576, H = 32, DH = 18, F = 4608;
constexpr int NQKV = 3 * D;
constexpr int NFEAT = 2 * DH;
constexpr int KVSZ = NFEAT * DH + NFEAT;

__device__ __forceinline__ float bf2f(u16 v) { return __uint_as_float(((unsigned)v) << 16); }
__device__ __forceinline__ u16 f2bf(float f) { __bf16 b = (__bf16)f; return __builtin_bit_cast(u16, b); }
__device__ __forceinline__ unsigned pack2(float lo, float hi) { return (unsigned)f2bf(lo) | ((unsigned)f2bf(hi) << 16); }
__device__ __forceinline__ float bfr(float f) { return bf2f(f2bf(f)); }
__device__ __forceinline__ float silu_f(float v) { return v * __builtin_amdgcn_rcpf(1.f + __builtin_amdgcn_exp2f(-1.4426950408889634f * v)); }

#define SBAR() __builtin_amdgcn_sched_barrier(0)
#define LBAR() do { asm volatile("s_waitcnt lgkmcnt(0)" ::: "memory"); __builtin_amdgcn_s_barrier(); asm volatile("" ::: "memory"); } while (0)
template <int N> __device__ __forceinline__ void waitv() { asm volatile("s_waitcnt vmcnt(%0)" ::"n"(N) : "memory"); }
__device__ __forceinline__ void rd128(u32x4& d, unsigned addr, int off) { asm volatile("ds_read_b128 %0, %1 offset:%2" : "=v"(d) : "v"(addr), "i"(off)); }
__device__ __forceinline__ void rdtr(u32x2& d, unsigned addr, int off) { asm volatile("ds_read_b64_tr_b16 %0, %1 offset:%2" : "=v"(d) : "v"(addr), "i"(off)); }

typedef __attribute__((ext_vector_type(2))) __bf16 bf16x2;
__device__ __forceinline__ float dot2(unsigned a, unsigned b, float c) { return __builtin_amdgcn_fdot2_f32_bf16(__builtin_bit_cast(bf16x2, a), __builtin_bit_cast(bf16x2, b), c, false); }
__device__ __forceinline__ void load_wa(const u16* __restrict__ wphi, int head, int lane, u32x4& wa0, u32x4& wa1) {
  const int fr = lane & 15, g = lane >> 4;
  const u16* w = wphi + head * (DH * DH) + fr;
  unsigned a[8], b[8];
#pragma unroll
  for (int j = 0; j < 8; ++j) {
    const int d = 8 * g + j;
    a[j] = d < DH ? w[d * DH] : 0u;
    b[j] = (d < DH && fr < 2) ? w[d * DH + 16] : 0u;
  }
#pragma unroll
  for (int e = 0; e < 4; ++e) { wa0[e] = a[2 * e] | (a[2 * e + 1] << 16); wa1[e] = b[2 * e] | (b[2 * e + 1] << 16); }
}

__device__ __forceinline__ float red16_32_max(float v) {
  auto r = __builtin_amdgcn_permlane16_swap(__float_as_uint(v), __float_as_uint(v), false, false);
  v = fmaxf(__uint_as_float(r[0]), __uint_as_float(r[1]));
  r = __builtin_amdgcn_permlane32_swap(__float_as_uint(v), __float_as_uint(v), false, false);
  return fmaxf(__uint_as_float(r[0]), __uint_as_float(r[1]));
}
__device__ __forceinline__ float red16_32_add(float v) {
  auto r = __builtin_amdgcn_permlane16_swap(__float_as_uint(v), __float_as_uint(v), false, false);
  v = __uint_as_float(r[0]) + __uint_as_float(r[1]);
  r = __builtin_amdgcn_permlane32_swap(__float_as_uint(v), __float_as_uint(v), false, false);
  return __uint_as_float(r[0]) + __uint_as_float(r[1]);
}
template <int WM, int WN, int MB, int NB, int BSUB, int NK, int QKVMAP, int LNF = 0>
__device__ __forceinline__ void gemm_core(const u16* __restrict__ A, int lda, const u16* __restrict__ B, int ldb, char* smem, f32x4 (&acc)[MB][NB]) {
  constexpr int BM = WM * MB * 16, NW = WM * WN;
#define CBMAP(wn_, nb_) (QKVMAP ? (nb_) * WN + (wn_) : (wn_) * NB + (nb_))
  constexpr int ABYTES = BM * 128, BBYTES = BSUB * 8192;
  constexpr int BOFF = 3 * ABYTES;
  constexpr int APW = BM / 8 / NW, BPW = BSUB * 8 / NW;
  static_assert((BM / 8) % NW == 0 && (BSUB * 8) % NW == 0, "pieces");
  int tid = threadIdx.x; asm volatile("" : "+v"(tid));
  const int lane = tid & 63, wid = __builtin_amdgcn_readfirstlane(tid >> 6);
  const int wm = wid / WN, wn = wid % WN;
  unsigned aoffs[APW], boffs[BPW];
  {
    const int r8 = lane >> 3, c = lane & 7;
#pragma unroll
    for (int p = 0; p < APW; ++p) {
      const int piece = p * NW + wid;
      aoffs[p] = (unsigned)((piece * 8 + r8) * lda + ((c ^ r8) << 3)) * 2u;
    }
#pragma unroll
    for (int p = 0; p < BPW; ++p) {
      const int pb = p * NW + wid, sub = pb >> 3, j = pb & 7;
      const int r = j * 8 + r8;
      const int swz = (((r >> 1) & 1) << 1) | ((r >> 3) & 1);
      const int chunk = (((c >> 1) ^ swz) << 1) | (c & 1);
      int col = sub * 64 + (chunk << 3);
      if (QKVMAP) {
        const int c8 = chunk << 3;
        col = c8 + (p == 2 && c8 >= 16 ? 432 : 0) + (p == 4 && c8 >= 32 ? 432 : 0) - (p == 6 && c8 >= 48 ? 16 : 0);
      }
      boffs[p] = (unsigned)(r * ldb + col) * 2u;
    }
  }
  auto stageA1 = [&](int slot, int p, int kt) {
    __builtin_amdgcn_global_load_lds((const AS1 void*)((const char*)(A + kt * 64 + (size_t)p * NW * 8 * lda) + aoffs[0]), (AS3 void*)(smem + slot * ABYTES + (p * NW + wid) * 1024), 16, 0, 0);
  };
  auto stageB1 = [&](int slot, int p, int kt) {
    __builtin_amdgcn_global_load_lds((const AS1 void*)((const char*)(B + (size_t)kt * 64 * ldb + (QKVMAP ? p * 64 + (p > 4 ? 864 : p > 2 ? 432 : 0) : ((p * NW) >> 3) * 64 + ((p * NW) & 7) * 8 * ldb)) + (QKVMAP ? boffs[p & 1 ? 0 : p] : boffs[0])), (AS3 void*)(smem + BOFF + slot * BBYTES + (p * NW + wid) * 1024), 16, 0, 0);
  };
#pragma unroll
  for (int p = 0; p < BPW; ++p) stageB1(0, p, 0);
#pragma unroll
  for (int p = 0; p < APW; ++p) stageA1(0, p, 0);
#pragma unroll
  for (int p = 0; p < APW; ++p) stageA1(1, p, 1);

  const int fr = lane & 15, g = lane >> 4, q = (lane & 15) >> 2, p4 = lane & 3;
  const unsigned sbase = (unsigned)(size_t)(AS3 char*)smem;
  constexpr bool AIMM = (2 * ABYTES + (MB - 1) * 2048) < 65536;
  unsigned aaddr[2][AIMM ? 1 : 3];
  {
    const int r = wm * MB * 16 + fr;
#pragma unroll
    for (int s = 0; s < (AIMM ? 1 : 3); ++s) {
      aaddr[0][s] = sbase + s * ABYTES + r * 128 + (((0 + g) ^ (r & 7)) << 4);
      aaddr[1][s] = sbase + s * ABYTES + r * 128 + (((4 + g) ^ (r & 7)) << 4);
    }
  }
  unsigned baddr[NB];
  {
    const int sB = (((q >> 1) & 1) << 1) | (g & 1);
    const int kr = 8 * g + q;
#pragma unroll
    for (int nb = 0; nb < NB; ++nb) {
      const int cb = CBMAP(wn, nb);
      baddr[nb] = sbase + BOFF + (QKVMAP ? nb * BBYTES : (cb >> 2) * 8192) + kr * 128 + ((((cb & 3) ^ sB)) << 5) + ((p4 >> 1) << 4) + 8 * (p4 & 1);
    }
  }
#pragma unroll
  for (int i = 0; i < MB; ++i)
#pragma unroll
    for (int j = 0; j < NB; ++j) acc[i][j] = (f32x4){0.f, 0.f, 0.f, 0.f};
  u32x4 af[2][MB];
  u32x2 bt[2][NB][2];
  constexpr int RPW = MB / WN > 0 ? MB / WN : 1, NBG = (NB + WM - 1) / WM;
  float rsm[RPW], rq[RPW], cs_[NBG];
#pragma unroll
  for (int i = 0; i < RPW; ++i) rsm[i] = rq[i] = 0.f;
#pragma unroll
  for (int j = 0; j < NBG; ++j) cs_[j] = 0.f;
  auto wait_set = [&](int set) {
#pragma unroll
    for (int i = 0; i < MB; ++i) asm volatile("" : "+v"(af[set][i]));
    asm volatile("s_waitcnt lgkmcnt(0)" ::: "memory");
#pragma unroll
    for (int i = 0; i < MB; ++i) asm volatile("" : "+v"(af[set][i]));
#pragma unroll
    for (int j = 0; j < NB; ++j) asm volatile("" : "+v"(bt[set][j][0]), "+v"(bt[set][j][1]));
  };
  constexpr int NRD = 2 * NB + MB;
  constexpr int NRS = NRD < MB * NB ? NRD : MB * NB;
#define PHASE(cs, rs, aslot, bslot, kk, do_reads, sb, sa, kb, ka)                                                                            \
  _Pragma("unroll") for (int i = 0; i < MB * NB; ++i) {                                                                              \
    const int mb = i / NB, nb = i % NB;                                                                                              \
    u32x4 bw = {bt[cs][nb][0][0], bt[cs][nb][0][1], bt[cs][nb][1][0], bt[cs][nb][1][1]};                                            \
    acc[mb][nb] = __builtin_amdgcn_mfma_f32_16x16x32_bf16(__builtin_bit_cast(bf16x8, bw), __builtin_bit_cast(bf16x8, af[cs][mb]), acc[mb][nb], 0, 0, 0); \
    SBAR();                                                                                                                          \
    if (LNF && i == 1) {                                                                                                             \
      if (LNF & 1) {                                                                             \
        _Pragma("unroll") for (int ii = 0; ii < RPW; ++ii) {                                                                         \
          u32x4 f = af[cs][ii];                                                                                                      \
          _Pragma("unroll") for (int w = 1; w < WN; ++w) f = (wn == w) ? af[cs][w * RPW + ii] : f;                                   \
          _Pragma("unroll") for (int e = 0; e < 4; ++e) { rsm[ii] = dot2(f[e], 0x3f803f80u, rsm[ii]); rq[ii] = dot2(f[e], f[e], rq[ii]); } \
        }                                                                                                                            \
      } else {                                                                                                                       \
        _Pragma("unroll") for (int w = 0; w < WN; ++w) if (wn == w) {                                                                \
          _Pragma("unroll") for (int ii = 0; ii < RPW; ++ii) _Pragma("unroll") for (int e = 0; e < 4; ++e) {                         \
            rsm[ii] = dot2(af[cs][w * RPW + ii][e], 0x3f803f80u, rsm[ii]); rq[ii] = dot2(af[cs][w * RPW + ii][e], af[cs][w * RPW + ii][e], rq[ii]); } \
        }                                                                                                                            \
      }                                                                                                                              \
      if (LNF & 4) {                                                                                                                 \
        _Pragma("unroll") for (int jj = 0; jj < NBG; ++jj) {                                                                         \
          u32x2 f0 = bt[cs][jj][0], f1 = bt[cs][jj][1];                                                                              \
          _Pragma("unroll") for (int w = 1; w < WM; ++w) { const int nbw = w * NBG + jj < NB ? w * NBG + jj : jj;                    \
            f0 = (wm == w) ? bt[cs][nbw][0] : f0; f1 = (wm == w) ? bt[cs][nbw][1] : f1; }                                            \
          cs_[jj] = dot2(f0[0], 0x3f803f80u, dot2(f0[1], 0x3f803f80u, dot2(f1[0], 0x3f803f80u, dot2(f1[1], 0x3f803f80u, cs_[jj])))); \
        }                                                                                                                            \
      } else {                                                                                                                       \
        _Pragma("unroll") for (int w = 0; w < WM; ++w) if (wm == w) {                                                                \
          _Pragma("unroll") for (int jj = 0; jj < NBG; ++jj) if (w * NBG + jj < NB) _Pragma("unroll") for (int e = 0; e < 4; ++e)    \
            cs_[jj] = dot2(bt[cs][w * NBG + jj][e >> 1][e & 1], 0x3f803f80u, cs_[jj]);                                               \
        }                                                                                                                            \
      }                                                                                                                              \
      SBAR();                                                                                                                        \
    }                                                                                                                                \
    if (do_reads) {                                                                                                                  \
      _Pragma("unroll") for (int r = (i < NRS ? (i * NRD) / NRS : NRD); r < (i < NRS ? ((i + 1) * NRD) / NRS : NRD); ++r) {          \
        if (r < 2 * NB) rdtr(bt[rs][r >> 1][r & 1], QKVMAP ? baddr[bslot] : baddr[r >> 1], (QKVMAP ? (r >> 1) * 8192 : (bslot) * BBYTES) + (kk) * 4096 + (r & 1) * 512);                  \
        else rd128(af[rs][r - 2 * NB], aaddr[kk][AIMM ? 0 : (aslot)], (AIMM ? (aslot) * ABYTES : 0) + (r - 2 * NB) * 2048);                                                       \
        SBAR();                                                                                                                      \
      }                                                                                                                              \
    }                                                                                                                                \
    _Pragma("unroll") for (int s = (i * (APW + BPW)) / (MB * NB); s < ((i + 1) * (APW + BPW)) / (MB * NB); ++s) {                  \
      if (s < BPW) { if ((sb) >= 0) stageB1(sb, s, kb); }                                                                                 \
      else { if ((sa) >= 0) stageA1(sa, s - BPW, ka); }                                                                                 \
      SBAR();                                                                                                                        \
    }                                                                                                                                \
  }
  waitv<APW>();
  __builtin_amdgcn_s_barrier();
#pragma unroll
  for (int nb = 0; nb < NB; ++nb) { rdtr(bt[0][nb][0], baddr[QKVMAP ? 0 : nb], QKVMAP ? nb * 8192 : 0); rdtr(bt[0][nb][1], baddr[QKVMAP ? 0 : nb], (QKVMAP ? nb * 8192 : 0) + 512); }
#pragma unroll
  for (int mb = 0; mb < MB; ++mb) rd128(af[0][mb], aaddr[0][0], mb * 2048);
#pragma unroll
  for (int it = 0; it < NK; ++it) {
    wait_set(0);
    SBAR();
    PHASE(0, 1, it % 3, it & 1, 1, true, it == 0 ? 1 : -1, it == 0 ? 2 : -1, 1, 2);
    wait_set(1);
    if (it + 2 < NK) waitv<APW>(); else waitv<0>();
    __builtin_amdgcn_s_barrier();
    SBAR();
    PHASE(1, 0, (it + 1) % 3, (it + 1) & 1, 0, it + 1 < NK, (it + 2 < NK) ? (it & 1) : -1, (it + 3 < NK) ? (it % 3) : -1, it + 2, it + 3);
  }
#undef PHASE
  if (LNF) {
    constexpr int BN = WN * NB * 16;
    float* st = (float*)smem;
#pragma unroll
    for (int i = 0; i < RPW; ++i) { rsm[i] = red16_32_add(rsm[i]); rq[i] = red16_32_add(rq[i]); }
#pragma unroll
    for (int j = 0; j < NBG; ++j) cs_[j] = red16_32_add(cs_[j]);
    __syncthreads();
    if (g == 0) {
#pragma unroll
      for (int i = 0; i < RPW; ++i) { const int r = (wm * MB + wn * RPW + i) * 16 + fr; st[r] = rsm[i]; st[BM + r] = rq[i]; }
#pragma unroll
      for (int j = 0; j < NBG; ++j) if (wm * NBG + j < NB) st[2 * BM + CBMAP(wn, wm * NBG + j) * 16 + fr] = cs_[j];
    }
    __syncthreads();
    float mean[MB], rstd[MB];
#pragma unroll
    for (int mb = 0; mb < MB; ++mb) {
      const int r = (wm * MB + mb) * 16 + fr;
      mean[mb] = st[r] * (1.f / D);
      rstd[mb] = rsqrtf(fmaxf(st[BM + r] * (1.f / D) - mean[mb] * mean[mb], 0.f) + 1e-5f);
    }
#pragma unroll
    for (int nb = 0; nb < NB; ++nb) {
      const f32x4 c4 = *(const f32x4*)(st + 2 * BM + CBMAP(wn, nb) * 16 + 4 * g);
#pragma unroll
      for (int mb = 0; mb < MB; ++mb)
#pragma unroll
        for (int e = 0; e < 4; ++e) acc[mb][nb][e] = rstd[mb] * (acc[mb][nb][e] - mean[mb] * c4[e]);
    }
  }
}

__device__ __forceinline__ void feat_mfma(u32x4 xb, u32x4 wa0, u32x4 wa1, bool g0, float (&e)[8], float (&ex)[4]) {
  f32x4 z = {0.f, 0.f, 0.f, 0.f};
  f32x4 p0 = __builtin_amdgcn_mfma_f32_16x16x32_bf16(__builtin_bit_cast(bf16x8, wa0), __builtin_bit_cast(bf16x8, xb), z, 0, 0, 0);
  f32x4 p1 = __builtin_amdgcn_mfma_f32_16x16x32_bf16(__builtin_bit_cast(bf16x8, wa1), __builtin_bit_cast(bf16x8, xb), z, 0, 0, 0);
  float p[4], q[2];
#pragma unroll
  for (int r = 0; r < 4; ++r) p[r] = bfr(p0[r]);
  q[0] = bfr(p1[0]); q[1] = bfr(p1[1]);
  float mx = fmaxf(fmaxf(p[0], p[1]), fmaxf(p[2], p[3])), mn = fminf(fminf(p[0], p[1]), fminf(p[2], p[3]));
  if (g0) { mx = fmaxf(mx, fmaxf(q[0], q[1])); mn = fminf(mn, fminf(q[0], q[1])); }
  mx = red16_32_max(mx);
  mn = -red16_32_max(-mn);
  float s1 = 0.f, s2 = 0.f;
#pragma unroll
  for (int r = 0; r < 4; ++r) { e[r] = __expf(p[r] - mx); e[4 + r] = __expf(mn - p[r]); s1 += e[r]; s2 += e[4 + r]; }
#pragma unroll
  for (int r = 0; r < 2; ++r) { ex[r] = g0 ? __expf(q[r] - mx) : 0.f; ex[2 + r] = g0 ? __expf(mn - q[r]) : 0.f; s1 += ex[r]; s2 += ex[2 + r]; }
  s1 = red16_32_add(s1);
  s2 = red16_32_add(s2);
  const float r1 = __builtin_amdgcn_rcpf(s1), r2 = __builtin_amdgcn_rcpf(s2);
#pragma unroll
  for (int r = 0; r < 4; ++r) { e[r] = e[r] * r1; e[4 + r] = e[4 + r] * r2; }
  ex[0] = ex[0] * r1; ex[1] = ex[1] * r1; ex[2] = ex[2] * r2; ex[3] = ex[3] * r2;
}

__global__ __launch_bounds__(512) void gemm1_kernel(const u16* __restrict__ xn, const u16* __restrict__ w1, const u16* __restrict__ wqkv,
                                                    const u16* __restrict__ wA, u16* __restrict__ h, u16* __restrict__ qbuf, float* __restrict__ kvpart) {
  extern __shared__ __attribute__((aligned(16))) char smem[];
  if (blockIdx.x < 192) {
    const int tm = blockIdx.x / 24, tn = blockIdx.x % 24;
    const int row0 = tm * 256, col0 = tn * 192;
    f32x4 acc[4][6];
    gemm_core<4, 2, 4, 6, 3, 9, 0, 2>(xn + (size_t)row0 * D, D, w1 + col0, F, smem, acc);
    int tid = threadIdx.x; asm volatile("" : "+v"(tid));
    const int lane = tid & 63, wid = tid >> 6, fr = lane & 15, g = lane >> 4;
    const int wm = wid >> 1, wn = wid & 1;
#pragma unroll
    for (int mb = 0; mb < 4; ++mb) {
      const int m = row0 + wm * 64 + mb * 16 + fr;
#pragma unroll
      for (int nb = 0; nb < 6; ++nb) {
        const int n = col0 + wn * 96 + nb * 16 + 4 * g;
        f32x4 v = acc[mb][nb];
        for (int r = 0; r < 4; ++r) v[r] = silu_f(bfr(v[r]));
        u32x2 w = {pack2(v[0], v[1]), pack2(v[2], v[3])};
        *(u32x2*)(h + (size_t)m * F + n) = w;
      }
    }
  } else {
    const int b = blockIdx.x - 192;
    const int tm = b >> 2, tn = b & 3;
    const int row0 = tm * 128;
    f32x4 acc[4][7];
    gemm_core<2, 4, 4, 7, 7, 9, 1, 7>(xn + (size_t)row0 * D, D, wqkv + tn * 144, NQKV, smem, acc);
    int tid = threadIdx.x; asm volatile("" : "+v"(tid));
    const int lane = tid & 63, wid = tid >> 6, fr = lane & 15, g = lane >> 4;
    const int wm = wid >> 2, wn = wid & 3;
    constexpr int TP = 296;
    u16* T = (u16*)smem;
    u16* PH = (u16*)(smem + 75776);
    u16* VV = (u16*)(smem + 75776 + 49152);
    u32x4 wfr[2][2];
    load_wa(wA, tn * 8 + (wid >> 1), lane, wfr[0][0], wfr[0][1]);
    load_wa(wA, tn * 8 + 4 + (wid >> 1), lane, wfr[1][0], wfr[1][1]);
    LBAR();
#pragma unroll
    for (int mb = 0; mb < 4; ++mb) {
      const int ml = wm * 64 + mb * 16 + fr;
#pragma unroll
      for (int nb = 0; nb < 7; ++nb) {
        const int cb = nb * 4 + wn;
        if (cb < 27) {
          f32x4 v = acc[mb][nb];
          u32x2 w = {pack2(v[0], v[1]), pack2(v[2], v[3])};
          if (cb < 9) *(u32x2*)(qbuf + (size_t)(row0 + ml) * D + tn * 144 + cb * 16 + 4 * g) = w;
          else *(u32x2*)(T + ml * TP + (cb - 9) * 16 + 4 * g) = w;
        }
      }
    }
#pragma unroll 1
    for (int hh = 0; hh < 2; ++hh) {
      LBAR();
#pragma unroll
      for (int u = 0; u < 4; ++u) {
        const int unit = wid * 4 + u, hl = unit >> 3, rb = unit & 7;
        const int head = hh * 4 + hl;
        const u32x4 wa0 = hh ? wfr[1][0] : wfr[0][0], wa1 = hh ? wfr[1][1] : wfr[0][1];
        const unsigned* kr = (const unsigned*)(T + (rb * 16 + fr) * TP + head * DH + 8 * g);
        const u32x4 xb = {kr[0], kr[1], kr[2], kr[3]};
        float e[8], ex[4];
        feat_mfma(xb, wa0, wa1, g == 0, e, ex);
        u16* pr = PH + (hl * 128 + rb * 16 + fr) * 48;
        *(u32x2*)(pr + 4 * g) = (u32x2){pack2(e[0], e[1]), pack2(e[2], e[3])};
        *(u32x2*)(pr + 16 + 4 * g) = (u32x2){pack2(e[4], e[5]), pack2(e[6], e[7])};
        if (g == 0) *(u32x2*)(pr + 32) = (u32x2){pack2(ex[0], ex[1]), pack2(ex[2], ex[3])};
        else *(u32x2*)(pr + 32 + 4 * g) = (u32x2){0u, 0u};
        const unsigned* vr = (const unsigned*)(T + (rb * 16 + fr) * TP + 144 + head * DH);
        u32x4 vv;
        if (g < 2) vv = (u32x4){vr[4 * g], vr[4 * g + 1], vr[4 * g + 2], vr[4 * g + 3]};
        else if (g == 2) vv = (u32x4){vr[8], 0x3f80u, 0u, 0u};
        else vv = (u32x4){0u, 0u, 0u, 0u};
        *(u32x4*)(VV + (hl * 128 + rb * 16 + fr) * 32 + 8 * g) = vv;
      }
      LBAR();
      if (wid < 4) {
        const int hl = wid;
        f32x4 a2[3][2];
#pragma unroll
        for (int i = 0; i < 3; ++i)
#pragma unroll
          for (int j = 0; j < 2; ++j) a2[i][j] = (f32x4){0.f, 0.f, 0.f, 0.f};
        const int q4 = (lane & 15) >> 2, p4 = lane & 3;
        const unsigned sb = (unsigned)(size_t)(AS3 char*)smem;
#pragma unroll
        for (int ks = 0; ks < 4; ++ks) {
          const int r0 = ks * 32 + 8 * g + q4;
          u32x2 pa[3][2], vb[2][2];
#pragma unroll
          for (int t = 0; t < 2; ++t) {
#pragma unroll
            for (int i = 0; i < 3; ++i) rdtr(pa[i][t], sb + 75776 + ((hl * 128 + r0 + 4 * t) * 48 + i * 16 + 4 * p4) * 2, 0);
#pragma unroll
            for (int j = 0; j < 2; ++j) rdtr(vb[j][t], sb + 75776 + 49152 + ((hl * 128 + r0 + 4 * t) * 32 + j * 16 + 4 * p4) * 2, 0);
          }
          asm volatile("s_waitcnt lgkmcnt(0)" ::: "memory");
#pragma unroll
          for (int i = 0; i < 3; ++i) { asm volatile("" : "+v"(pa[i][0]), "+v"(pa[i][1])); }
#pragma unroll
          for (int j = 0; j < 2; ++j) { asm volatile("" : "+v"(vb[j][0]), "+v"(vb[j][1])); }
#pragma unroll
          for (int i = 0; i < 3; ++i)
#pragma unroll
            for (int j = 0; j < 2; ++j) {
              u32x4 aw = {pa[i][0][0], pa[i][0][1], pa[i][1][0], pa[i][1][1]};
              u32x4 bw = {vb[j][0][0], vb[j][0][1], vb[j][1][0], vb[j][1][1]};
              a2[i][j] = __builtin_amdgcn_mfma_f32_16x16x32_bf16(__builtin_bit_cast(bf16x8, aw), __builtin_bit_cast(bf16x8, bw), a2[i][j], 0, 0, 0);
            }
        }
        float* kvp = kvpart + ((size_t)tm * H + tn * 8 + hh * 4 + hl) * (48 * 32);
#pragma unroll
        for (int i = 0; i < 3; ++i)
#pragma unroll
          for (int j = 0; j < 2; ++j)
#pragma unroll
            for (int r = 0; r < 4; ++r) kvp[(i * 16 + 4 * g + r) * 32 + j * 16 + fr] = a2[i][j][r];
      }
    }
  }
}

__device__ __forceinline__ void o_step(const u16* __restrict__ qbuf, const u16* __restrict__ wA, const float* __restrict__ kvpart, u16* __restrict__ o,
                                       int h, int rq, char* smem) {
  const int tid = threadIdx.x, lane = tid & 63, wid = tid >> 6, fr = lane & 15, g = lane >> 4;
  float* kvs = (float*)smem;
  for (int i = tid; i < 48 * 32; i += 512) {
    float a = 0.f;
#pragma unroll
    for (int t = 0; t < 16; ++t) a += kvpart[((size_t)t * H + h) * (48 * 32) + i];
    kvs[i] = bfr(a);
  }
  __syncthreads();
  u32x4 ka[2][2];
#pragma unroll
  for (int j = 0; j < 2; ++j) {
    const int d = fr + 16 * j;
#pragma unroll
    for (int e = 0; e < 4; ++e) {
      ka[0][j][e] = pack2(kvs[(e < 2 ? 4 * g + 2 * e : 16 + 4 * g + 2 * (e - 2)) * 32 + d], kvs[(e < 2 ? 4 * g + 2 * e + 1 : 16 + 4 * g + 2 * (e - 2) + 1) * 32 + d]);
      ka[1][j][e] = (g == 0 && e < 2) ? pack2(kvs[(32 + 2 * e) * 32 + d], kvs[(33 + 2 * e) * 32 + d]) : 0u;
    }
  }
  u32x4 wa0, wa1;
  load_wa(wA, h, lane, wa0, wa1);
  u32x4 xq[8];
#pragma unroll
  for (int u = 0; u < 8; ++u) {
    const int row = rq * 1024 + wid * 128 + u * 16 + fr;
    const unsigned* qr = (const unsigned*)(qbuf + (size_t)row * D + h * DH + 8 * g);
    if (h == H - 1 && g >= 2) xq[u] = (g == 2) ? (u32x4){qr[0], 0u, 0u, 0u} : (u32x4){0u, 0u, 0u, 0u};
    else xq[u] = (u32x4){qr[0], qr[1], qr[2], qr[3]};
  }
#pragma unroll
  for (int u = 0; u < 8; ++u) {
    const int row = rq * 1024 + wid * 128 + u * 16 + fr;
    const u32x4 xb = xq[u];
    float e[8], ex[4];
    feat_mfma(xb, wa0, wa1, g == 0, e, ex);
    const u32x4 pb0 = {pack2(e[0], e[1]), pack2(e[2], e[3]), pack2(e[4], e[5]), pack2(e[6], e[7])};
    const u32x4 pb1 = {pack2(ex[0], ex[1]), pack2(ex[2], ex[3]), 0u, 0u};
    f32x4 n[2];
#pragma unroll
    for (int j = 0; j < 2; ++j) {
      f32x4 z = {0.f, 0.f, 0.f, 0.f};
      z = __builtin_amdgcn_mfma_f32_16x16x32_bf16(__builtin_bit_cast(bf16x8, ka[0][j]), __builtin_bit_cast(bf16x8, pb0), z, 0, 0, 0);
      n[j] = __builtin_amdgcn_mfma_f32_16x16x32_bf16(__builtin_bit_cast(bf16x8, ka[1][j]), __builtin_bit_cast(bf16x8, pb1), z, 0, 0, 0);
    }
    float den = g == 0 ? n[1][2] : 0.f;
    den = red16_32_add(den);
    den = bfr(bfr(den) + 1e-6f);
    const float rden = __builtin_amdgcn_rcpf(den);
    unsigned* orow = (unsigned*)(o + (size_t)row * D + h * DH);
    orow[2 * g] = pack2(bfr(n[0][0]) * rden, bfr(n[0][1]) * rden);
    orow[2 * g + 1] = pack2(bfr(n[0][2]) * rden, bfr(n[0][3]) * rden);
    if (g == 0) orow[8] = pack2(bfr(n[1][0]) * rden, bfr(n[1][1]) * rden);
  }
}

__global__ __launch_bounds__(512) void gemm2_kernel(const u16* __restrict__ h, const u16* __restrict__ w2, float* __restrict__ part,
                                                    const u16* __restrict__ qbuf, const u16* __restrict__ wA, const float* __restrict__ kvpart,
                                                    u16* __restrict__ o) {
  extern __shared__ __attribute__((aligned(16))) char smem[];
  const int tid = threadIdx.x, lane = tid & 63, wid = tid >> 6;
  if (blockIdx.x >= 192) {
    const int b = blockIdx.x - 192;
    o_step(qbuf, wA, kvpart, o, b >> 1, b & 1, smem);
    return;
  }
  const int fr = lane & 15, g = lane >> 4;
  const int split = blockIdx.x & 3, tile = blockIdx.x >> 2;
  const int tm = tile / 3, tn = tile % 3;
  const int row0 = tm * 128, col0 = tn * 192, k0 = split * 1152;
  f32x4 acc[2][6];
  gemm_core<4, 2, 2, 6, 3, 18, 0>(h + (size_t)row0 * F + k0, F, w2 + (size_t)k0 * D + col0, D, smem, acc);
  const int wm = wid >> 1, wn = wid & 1;
  u16* P = (u16*)part + (size_t)split * S * D;
#pragma unroll
  for (int mb = 0; mb < 2; ++mb) {
    const int m = row0 + wm * 32 + mb * 16 + fr;
#pragma unroll
    for (int nb = 0; nb < 6; ++nb) {
      const int n = col0 + wn * 96 + nb * 16 + 4 * g;
      *(u32x2*)(P + (size_t)m * D + n) = (u32x2){pack2(acc[mb][nb][0], acc[mb][nb][1]), pack2(acc[mb][nb][2], acc[mb][nb][3])};
    }
  }
}

__global__ __launch_bounds__(256) void final_kernel(const u16* __restrict__ x, const u16* __restrict__ o, const u16* __restrict__ wo,
                                                    const float* __restrict__ part, u16* __restrict__ out, int nsplit) {
  extern __shared__ __attribute__((aligned(16))) char smem[];
  const int tid = threadIdx.x, lane = tid & 63, wid = tid >> 6;
  const int fr = lane & 15, g = lane >> 4;
  const int tm = blockIdx.x / 9, tn = blockIdx.x % 9;
  const int row0 = tm * 64, col0 = tn * 64;
  const int wm = wid >> 1, wn = wid & 1;
  float res[2][2][4];
#pragma unroll
  for (int mb = 0; mb < 2; ++mb)
#pragma unroll
    for (int nb = 0; nb < 2; ++nb) {
      const int m = row0 + wm * 32 + mb * 16 + fr, n = col0 + wn * 32 + nb * 16 + 4 * g;
      f32x4 a = {0.f, 0.f, 0.f, 0.f};
#pragma unroll
      for (int sp = 0; sp < 4; ++sp) { const u32x2 p = *(const u32x2*)((const u16*)part + (size_t)sp * S * D + (size_t)m * D + n);
        a += (f32x4){__uint_as_float(p[0] << 16), __uint_as_float(p[0] & 0xffff0000u), __uint_as_float(p[1] << 16), __uint_as_float(p[1] & 0xffff0000u)}; }
      for (int r = 0; r < 4; ++r) res[mb][nb][r] = a[r];
    }
#pragma unroll
  for (int mb = 0; mb < 2; ++mb)
#pragma unroll
    for (int nb = 0; nb < 2; ++nb)
      for (int r = 0; r < 4; ++r) asm volatile("" : "+v"(res[mb][nb][r]));
  f32x4 acc[2][2];
  gemm_core<2, 2, 2, 2, 1, 9, 0>(o + (size_t)row0 * D, D, wo + col0, D, smem, acc);
#pragma unroll
  for (int mb = 0; mb < 2; ++mb)
#pragma unroll
    for (int nb = 0; nb < 2; ++nb) {
      const int m = row0 + wm * 32 + mb * 16 + fr, n = col0 + wn * 32 + nb * 16 + 4 * g;
      u32x2 xv = *(const u32x2*)(x + (size_t)m * D + n);
      float xs[4] = {__uint_as_float(xv[0] << 16), __uint_as_float(xv[0] & 0xffff0000u), __uint_as_float(xv[1] << 16), __uint_as_float(xv[1] & 0xffff0000u)};
      float v[4];
      for (int r = 0; r < 4; ++r) v[r] = bfr(xs[r] + bfr(acc[mb][nb][r])) + bfr(res[mb][nb][r]);
      u32x2 w = {pack2(v[0], v[1]), pack2(v[2], v[3])};
      *(u32x2*)(out + (size_t)m * D + n) = w;
    }
}

extern "C" void kernel_launch(void* const* d_in, const int* in_sizes, int n_in, void* d_out, int out_size, void* d_ws, size_t ws_size,
                              hipStream_t stream) {
  const u16* x = (const u16*)d_in[0];
  const u16* wqkv = (const u16*)d_in[1];
  const u16* wphi = (const u16*)d_in[2];
  const u16* wo = (const u16*)d_in[3];
  const u16* w1 = (const u16*)d_in[5];
  const u16* w2 = (const u16*)d_in[6];
  u16* out = (u16*)d_out;
  char* ws = (char*)d_ws;
  size_t off = 0;
  auto take = [&](size_t bytes) { char* p = ws + off; off += (bytes + 255) & ~(size_t)255; return p; };
  u16* qbuf = (u16*)take((size_t)S * D * 2);
  u16* h = (u16*)take((size_t)S * F * 2);
  u16* o = (u16*)take((size_t)S * D * 2);
  float* kvpart = (float*)take((size_t)16 * H * 48 * 32 * 4);
  const u16* wA = wphi;
  const u16* xn = x;
  constexpr int NSPLIT = 4;
  float* part = (float*)take((size_t)NSPLIT * S * D * 4);

  const int shm1 = 160 * 1024, shm2 = (3 * 128 + 2 * 192) * 128;
  hipFuncSetAttribute((const void*)gemm1_kernel, hipFuncAttributeMaxDynamicSharedMemorySize, shm1);
  hipFuncSetAttribute((const void*)gemm2_kernel, hipFuncAttributeMaxDynamicSharedMemorySize, shm2);
  gemm1_kernel<<<256, 512, shm1, stream>>>(xn, w1, wqkv, wA, h, qbuf, kvpart);
  gemm2_kernel<<<192 + 64, 512, shm2, stream>>>(h, w2, part, qbuf, wA, kvpart, o);
  final_kernel<<<32 * 9, 256, (3 * 64 + 2 * 64) * 128, stream>>>(x, o, wo, part, out, NSPLIT);
}
```

```cpp
#include <hip/hip_runtime.h>
#include <hip/hip_bf16.h>

typedef unsigned short u16;
typedef __attribute__((ext_vector_type(8))) __bf16 bf16x8;
typedef __attribute__((ext_vector_type(4))) float f32x4;
typedef __attribute__((ext_vector_type(4))) unsigned u32x4;
typedef __attribute__((ext_vector_type(2))) unsigned u32x2;
#define AS1 __attribute__((address_space(1)))
#define AS3 __attribute__((address_space(3)))
typedef __attribute__((ext_vector_type(2))) float f32x2;
typedef const __attribute__((address_space(4))) float* cfp;

constexpr int S = 2048, D = 576, H = 32, DH = 18, F = 4608;
constexpr int NQKV = 3 * D;
constexpr int NFEAT = 2 * DH;
constexpr int KVSZ = NFEAT * DH + NFEAT;

__device__ __forceinline__ float bf2f(u16 v) { return __uint_as_float(((unsigned)v) << 16); }
__device__ __forceinline__ u16 f2bf(float f) { __bf16 b = (__bf16)f; return __builtin_bit_cast(u16, b); }
__device__ __forceinline__ unsigned pack2(float lo, float hi) { return (unsigned)f2bf(lo) | ((unsigned)f2bf(hi) << 16); }
__device__ __forceinline__ float bfr(float f) { return bf2f(f2bf(f)); }
__device__ __forceinline__ float silu_f(float v) { return v * __builtin_amdgcn_rcpf(1.f + __builtin_amdgcn_exp2f(-1.4426950408889634f * v)); }

#define SBAR() __builtin_amdgcn_sched_barrier(0)
#define LBAR() do { asm volatile("s_waitcnt lgkmcnt(0)" ::: "memory"); __builtin_amdgcn_s_barrier(); asm volatile("" ::: "memory"); } while (0)
template <int N> __device__ __forceinline__ void waitv() { asm volatile("s_waitcnt vmcnt(%0)" ::"n"(N) : "memory"); }
__device__ __forceinline__ void rd128(u32x4& d, unsigned addr, int off) { asm volatile("ds_read_b128 %0, %1 offset:%2" : "=v"(d) : "v"(addr), "i"(off)); }
__device__ __forceinline__ void rdtr(u32x2& d, unsigned addr, int off) { asm volatile("ds_read_b64_tr_b16 %0, %1 offset:%2" : "=v"(d) : "v"(addr), "i"(off)); }

typedef __attribute__((ext_vector_type(2))) __bf16 bf16x2;
__device__ __forceinline__ float dot2(unsigned a, unsigned b, float c) { return __builtin_amdgcn_fdot2_f32_bf16(__builtin_bit_cast(bf16x2, a), __builtin_bit_cast(bf16x2, b), c, false); }
__device__ __forceinline__ void load_wa(const u16* __restrict__ wphi, int head, int lane, u32x4& wa0, u32x4& wa1) {
  const int fr = lane & 15, g = lane >> 4;
  const u16* w = wphi + head * (DH * DH) + fr;
  unsigned a[8], b[8];
#pragma unroll
  for (int j = 0; j < 8; ++j) {
    const int d = 8 * g + j;
    a[j] = d < DH ? w[d * DH] : 0u;
    b[j] = (d < DH && fr < 2) ? w[d * DH + 16] : 0u;
  }
#pragma unroll
  for (int e = 0; e < 4; ++e) { wa0[e] = a[2 * e] | (a[2 * e + 1] << 16); wa1[e] = b[2 * e] | (b[2 * e + 1] << 16); }
}

__device__ __forceinline__ float red16_32_max(float v) {
  auto r = __builtin_amdgcn_permlane16_swap(__float_as_uint(v), __float_as_uint(v), false, false);
  v = fmaxf(__uint_as_float(r[0]), __uint_as_float(r[1]));
  r = __builtin_amdgcn_permlane32_swap(__float_as_uint(v), __float_as_uint(v), false, false);
  return fmaxf(__uint_as_float(r[0]), __uint_as_float(r[1]));
}
__device__ __forceinline__ float red16_32_add(float v) {
  auto r = __builtin_amdgcn_permlane16_swap(__float_as_uint(v), __float_as_uint(v), false, false);
  v = __uint_as_float(r[0]) + __uint_as_float(r[1]);
  r = __builtin_amdgcn_permlane32_swap(__float_as_uint(v), __float_as_uint(v), false, false);
  return __uint_as_float(r[0]) + __uint_as_float(r[1]);
}
template <int WM, int WN, int MB, int NB, int BSUB, int NK, int QKVMAP, int LNF = 0>
__device__ __forceinline__ void gemm_core(const u16* __restrict__ A, int lda, const u16* __restrict__ B, int ldb, char* smem, f32x4 (&acc)[MB][NB]) {
  constexpr int BM = WM * MB * 16, NW = WM * WN;
#define CBMAP(wn_, nb_) (QKVMAP ? (nb_) * WN + (wn_) : (wn_) * NB + (nb_))
  constexpr int ABYTES = BM * 128, BBYTES = BSUB * 8192;
  constexpr int BOFF = 3 * ABYTES;
  constexpr int APW = BM / 8 / NW, BPW = BSUB * 8 / NW;
  static_assert((BM / 8) % NW == 0 && (BSUB * 8) % NW == 0, "pieces");
  int tid = threadIdx.x; asm volatile("" : "+v"(tid));
  const int lane = tid & 63, wid = __builtin_amdgcn_readfirstlane(tid >> 6);
  const int wm = wid / WN, wn = wid % WN;
  unsigned aoffs[APW], boffs[BPW];
  {
    const int r8 = lane >> 3, c = lane & 7;
#pragma unroll
    for (int p = 0; p < APW; ++p) {
      const int piece = p * NW + wid;
      aoffs[p] = (unsigned)((piece * 8 + r8) * lda + ((c ^ r8) << 3)) * 2u;
    }
#pragma unroll
    for (int p = 0; p < BPW; ++p) {
      const int pb = p * NW + wid, sub = pb >> 3, j = pb & 7;
      const int r = j * 8 + r8;
      const int swz = (((r >> 1) & 1) << 1) | ((r >> 3) & 1);
      const int chunk = (((c >> 1) ^ swz) << 1) | (c & 1);
      int col = sub * 64 + (chunk << 3);
      if (QKVMAP) {
        const int c8 = chunk << 3;
        col = c8 + (p == 2 && c8 >= 16 ? 432 : 0) + (p == 4 && c8 >= 32 ? 432 : 0) - (p == 6 && c8 >= 48 ? 16 : 0);
      }
      boffs[p] = (unsigned)(r * ldb + col) * 2u;
    }
  }
  auto stageA1 = [&](int slot, int p, int kt) {
    __builtin_amdgcn_global_load_lds((const AS1 void*)((const char*)(A + kt * 64 + (size_t)p * NW * 8 * lda) + aoffs[0]), (AS3 void*)(smem + slot * ABYTES + (p * NW + wid) * 1024), 16, 0, 0);
  };
  auto stageB1 = [&](int slot, int p, int kt) {
    __builtin_amdgcn_global_load_lds((const AS1 void*)((const char*)(B + (size_t)kt * 64 * ldb + (QKVMAP ? p * 64 + (p > 4 ? 864 : p > 2 ? 432 : 0) : ((p * NW) >> 3) * 64 + ((p * NW) & 7) * 8 * ldb)) + (QKVMAP ? boffs[p & 1 ? 0 : p] : boffs[0])), (AS3 void*)(smem + BOFF + slot * BBYTES + (p * NW + wid) * 1024), 16, 0, 0);
  };
#pragma unroll
  for (int p = 0; p < BPW; ++p) stageB1(0, p, 0);
#pragma unroll
  for (int p = 0; p < APW; ++p) stageA1(0, p, 0);
#pragma unroll
  for (int p = 0; p < APW; ++p) stageA1(1, p, 1);

  const int fr = lane & 15, g = lane >> 4, q = (lane & 15) >> 2, p4 = lane & 3;
  const unsigned sbase = (unsigned)(size_t)(AS3 char*)smem;
  constexpr bool AIMM = (2 * ABYTES + (MB - 1) * 2048) < 65536;
  unsigned aaddr[2][AIMM ? 1 : 3];
  {
    const int r = wm * MB * 16 + fr;
#pragma unroll
    for (int s = 0; s < (AIMM ? 1 : 3); ++s) {
      aaddr[0][s] = sbase + s * ABYTES + r * 128 + (((0 + g) ^ (r & 7)) << 4);
      aaddr[1][s] = sbase + s * ABYTES + r * 128 + (((4 + g) ^ (r & 7)) << 4);
    }
  }
  unsigned baddr[NB];
  {
    const int sB = (((q >> 1) & 1) << 1) | (g & 1);
    const int kr = 8 * g + q;
#pragma unroll
    for (int nb = 0; nb < NB; ++nb) {
      const int cb = CBMAP(wn, nb);
      baddr[nb] = sbase + BOFF + (QKVMAP ? nb * BBYTES : (cb >> 2) * 8192) + kr * 128 + ((((cb & 3) ^ sB)) << 5) + ((p4 >> 1) << 4) + 8 * (p4 & 1);
    }
  }
#pragma unroll
  for (int i = 0; i < MB; ++i)
#pragma unroll
    for (int j = 0; j < NB; ++j) acc[i][j] = (f32x4){0.f, 0.f, 0.f, 0.f};
  u32x4 af[2][MB];
  u32x2 bt[2][NB][2];
  constexpr int RPW = MB / WN > 0 ? MB / WN : 1, NBG = (NB + WM - 1) / WM;
  float rsm[RPW], rq[RPW], cs_[NBG];
#pragma unroll
  for (int i = 0; i < RPW; ++i) rsm[i] = rq[i] = 0.f;
#pragma unroll
  for (int j = 0; j < NBG; ++j) cs_[j] = 0.f;
  auto wait_set = [&](int set) {
#pragma unroll
    for (int i = 0; i < MB; ++i) asm volatile("" : "+v"(af[set][i]));
    asm volatile("s_waitcnt lgkmcnt(0)" ::: "memory");
#pragma unroll
    for (int i = 0; i < MB; ++i) asm volatile("" : "+v"(af[set][i]));
#pragma unroll
    for (int j = 0; j < NB; ++j) asm volatile("" : "+v"(bt[set][j][0]), "+v"(bt[set][j][1]));
  };
  constexpr int NRD = 2 * NB + MB;
  constexpr int NRS = NRD < MB * NB ? NRD : MB * NB;
#define PHASE(cs, rs, aslot, bslot, kk, do_reads, sb, sa, kb, ka)                                                                            \
  _Pragma("unroll") for (int i = 0; i < MB * NB; ++i) {                                                                              \
    const int mb = i / NB, nb = i % NB;                                                                                              \
    u32x4 bw = {bt[cs][nb][0][0], bt[cs][nb][0][1], bt[cs][nb][1][0], bt[cs][nb][1][1]};                                            \
    acc[mb][nb] = __builtin_amdgcn_mfma_f32_16x16x32_bf16(__builtin_bit_cast(bf16x8, bw), __builtin_bit_cast(bf16x8, af[cs][mb]), acc[mb][nb], 0, 0, 0); \
    SBAR();                                                                                                                          \
    if (LNF && i == 1) {                                                                                                             \
      if (LNF & 1) {                                                                             \
        _Pragma("unroll") for (int ii = 0; ii < RPW; ++ii) {                                                                         \
          u32x4 f = af[cs][ii];                                                                                                      \
          _Pragma("unroll") for (int w = 1; w < WN; ++w) f = (wn == w) ? af[cs][w * RPW + ii] : f;                                   \
          _Pragma("unroll") for (int e = 0; e < 4; ++e) { rsm[ii] = dot2(f[e], 0x3f803f80u, rsm[ii]); rq[ii] = dot2(f[e], f[e], rq[ii]); } \
        }                                                                                                                            \
      } else {                                                                                                                       \
        _Pragma("unroll") for (int w = 0; w < WN; ++w) if (wn == w) {                                                                \
          _Pragma("unroll") for (int ii = 0; ii < RPW; ++ii) _Pragma("unroll") for (int e = 0; e < 4; ++e) {                         \
            rsm[ii] = dot2(af[cs][w * RPW + ii][e], 0x3f803f80u, rsm[ii]); rq[ii] = dot2(af[cs][w * RPW + ii][e], af[cs][w * RPW + ii][e], rq[ii]); } \
        }                                                                                                                            \
      }                                                                                                                              \
      if (LNF & 4) {                                                                                                                 \
        _Pragma("unroll") for (int jj = 0; jj < NBG; ++jj) {                                                                         \
          u32x2 f0 = bt[cs][jj][0], f1 = bt[cs][jj][1];                                                                              \
          _Pragma("unroll") for (int w = 1; w < WM; ++w) { const int nbw = w * NBG + jj < NB ? w * NBG + jj : jj;                    \
            f0 = (wm == w) ? bt[cs][nbw][0] : f0; f1 = (wm == w) ? bt[cs][nbw][1] : f1; }                                            \
          cs_[jj] = dot2(f0[0], 0x3f803f80u, dot2(f0[1], 0x3f803f80u, dot2(f1[0], 0x3f803f80u, dot2(f1[1], 0x3f803f80u, cs_[jj])))); \
        }                                                                                                                            \
      } else {                                                                                                                       \
        _Pragma("unroll") for (int w = 0; w < WM; ++w) if (wm == w) {                                                                \
          _Pragma("unroll") for (int jj = 0; jj < NBG; ++jj) if (w * NBG + jj < NB) _Pragma("unroll") for (int e = 0; e < 4; ++e)    \
            cs_[jj] = dot2(bt[cs][w * NBG + jj][e >> 1][e & 1], 0x3f803f80u, cs_[jj]);                                               \
        }                                                                                                                            \
      }                                                                                                                              \
      SBAR();                                                                                                                        \
    }                                                                                                                                \
    if (do_reads) {                                                                                                                  \
      _Pragma("unroll") for (int r = (i < NRS ? (i * NRD) / NRS : NRD); r < (i < NRS ? ((i + 1) * NRD) / NRS : NRD); ++r) {          \
        if (r < 2 * NB) rdtr(bt[rs][r >> 1][r & 1], QKVMAP ? baddr[bslot] : baddr[r >> 1], (QKVMAP ? (r >> 1) * 8192 : (bslot) * BBYTES) + (kk) * 4096 + (r & 1) * 512);                  \
        else rd128(af[rs][r - 2 * NB], aaddr[kk][AIMM ? 0 : (aslot)], (AIMM ? (aslot) * ABYTES : 0) + (r - 2 * NB) * 2048);                                                       \
        SBAR();                                                                                                                      \
      }                                                                                                                              \
    }                                                                                                                                \
    _Pragma("unroll") for (int s = (i * (APW + BPW)) / (MB * NB); s < ((i + 1) * (APW + BPW)) / (MB * NB); ++s) {                  \
      if (s < BPW) { if ((sb) >= 0) stageB1(sb, s, kb); }                                                                                 \
      else { if ((sa) >= 0) stageA1(sa, s - BPW, ka); }                                                                                 \
      SBAR();                                                                                                                        \
    }                                                                                                                                \
  }
  waitv<APW>();
  __builtin_amdgcn_s_barrier();
#pragma unroll
  for (int nb = 0; nb < NB; ++nb) { rdtr(bt[0][nb][0], baddr[QKVMAP ? 0 : nb], QKVMAP ? nb * 8192 : 0); rdtr(bt[0][nb][1], baddr[QKVMAP ? 0 : nb], (QKVMAP ? nb * 8192 : 0) + 512); }
#pragma unroll
  for (int mb = 0; mb < MB; ++mb) rd128(af[0][mb], aaddr[0][0], mb * 2048);
#pragma unroll
  for (int it = 0; it < NK; ++it) {
    wait_set(0);
    SBAR();
    PHASE(0, 1, it % 3, it & 1, 1, true, it == 0 ? 1 : -1, it == 0 ? 2 : -1, 1, 2);
    wait_set(1);
    if (it + 2 < NK) waitv<APW>(); else waitv<0>();
    __builtin_amdgcn_s_barrier();
    SBAR();
    PHASE(1, 0, (it + 1) % 3, (it + 1) & 1, 0, it + 1 < NK, (it + 2 < NK) ? (it & 1) : -1, (it + 3 < NK) ? (it % 3) : -1, it + 2, it + 3);
  }
#undef PHASE
  if (LNF) {
    constexpr int BN = WN * NB * 16;
    float* st = (float*)smem;
#pragma unroll
    for (int i = 0; i < RPW; ++i) { rsm[i] = red16_32_add(rsm[i]); rq[i] = red16_32_add(rq[i]); }
#pragma unroll
    for (int j = 0; j < NBG; ++j) cs_[j] = red16_32_add(cs_[j]);
    __syncthreads();
    if (g == 0) {
#pragma unroll
      for (int i = 0; i < RPW; ++i) { const int r = (wm * MB + wn * RPW + i) * 16 + fr; st[r] = rsm[i]; st[BM + r] = rq[i]; }
#pragma unroll
      for (int j = 0; j < NBG; ++j) if (wm * NBG + j < NB) st[2 * BM + CBMAP(wn, wm * NBG + j) * 16 + fr] = cs_[j];
    }
    __syncthreads();
    float mean[MB], rstd[MB];
#pragma unroll
    for (int mb = 0; mb < MB; ++mb) {
      const int r = (wm * MB + mb) * 16 + fr;
      mean[mb] = st[r] * (1.f / D);
      rstd[mb] = rsqrtf(fmaxf(st[BM + r] * (1.f / D) - mean[mb] * mean[mb], 0.f) + 1e-5f);
    }
#pragma unroll
    for (int nb = 0; nb < NB; ++nb) {
      const f32x4 c4 = *(const f32x4*)(st + 2 * BM + CBMAP(wn, nb) * 16 + 4 * g);
#pragma unroll
      for (int mb = 0; mb < MB; ++mb)
#pragma unroll
        for (int e = 0; e < 4; ++e) acc[mb][nb][e] = rstd[mb] * (acc[mb][nb][e] - mean[mb] * c4[e]);
    }
  }
}

__device__ __forceinline__ void feat_mfma(u32x4 xb, u32x4 wa0, u32x4 wa1, bool g0, float (&e)[8], float (&ex)[4]) {
  f32x4 z = {0.f, 0.f, 0.f, 0.f};
  f32x4 p0 = __builtin_amdgcn_mfma_f32_16x16x32_bf16(__builtin_bit_cast(bf16x8, wa0), __builtin_bit_cast(bf16x8, xb), z, 0, 0, 0);
  f32x4 p1 = __builtin_amdgcn_mfma_f32_16x16x32_bf16(__builtin_bit_cast(bf16x8, wa1), __builtin_bit_cast(bf16x8, xb), z, 0, 0, 0);
  float p[4], q[2];
#pragma unroll
  for (int r = 0; r < 4; ++r) p[r] = bfr(p0[r]);
  q[0] = bfr(p1[0]); q[1] = bfr(p1[1]);
  float mx = fmaxf(fmaxf(p[0], p[1]), fmaxf(p[2], p[3])), mn = fminf(fminf(p[0], p[1]), fminf(p[2], p[3]));
  if (g0) { mx = fmaxf(mx, fmaxf(q[0], q[1])); mn = fminf(mn, fminf(q[0], q[1])); }
  mx = red16_32_max(mx);
  mn = -red16_32_max(-mn);
  float s1 = 0.f, s2 = 0.f;
#pragma unroll
  for (int r = 0; r < 4; ++r) { e[r] = __expf(p[r] - mx); e[4 + r] = __expf(mn - p[r]); s1 += e[r]; s2 += e[4 + r]; }
#pragma unroll
  for (int r = 0; r < 2; ++r) { ex[r] = g0 ? __expf(q[r] - mx) : 0.f; ex[2 + r] = g0 ? __expf(mn - q[r]) : 0.f; s1 += ex[r]; s2 += ex[2 + r]; }
  s1 = red16_32_add(s1);
  s2 = red16_32_add(s2);
  const float r1 = __builtin_amdgcn_rcpf(s1), r2 = __builtin_amdgcn_rcpf(s2);
#pragma unroll
  for (int r = 0; r < 4; ++r) { e[r] = e[r] * r1; e[4 + r] = e[4 + r] * r2; }
  ex[0] = ex[0] * r1; ex[1] = ex[1] * r1; ex[2] = ex[2] * r2; ex[3] = ex[3] * r2;
}

__global__ __launch_bounds__(512) void gemm1_kernel(const u16* __restrict__ xn, const u16* __restrict__ w1, const u16* __restrict__ wqkv,
                                                    const u16* __restrict__ wA, u16* __restrict__ h, u16* __restrict__ qbuf, float* __restrict__ kvpart,
                                                    const u16* __restrict__ w2, const u16* __restrict__ wo) {
  extern __shared__ __attribute__((aligned(16))) char smem[];
  if (blockIdx.x < 192) {
    const int tm = blockIdx.x / 24, tn = blockIdx.x % 24;
    const int row0 = tm * 256, col0 = tn * 192;
    {
      const int t = threadIdx.x;
      AS3 void* junk = (AS3 void*)(smem + 147456 + (t >> 6) * 256);
      if (t < 216) __builtin_amdgcn_global_load_lds((const AS1 void*)(w1 + (size_t)(tm * 72 + t / 3) * F + col0 + (t % 3) * 64), junk, 4, 0, 0);
      else if (t < 216 + 216) __builtin_amdgcn_global_load_lds((const AS1 void*)(w2 + (size_t)blockIdx.x * 13824 + (t - 216) * 64), junk, 4, 0, 0);
      else if (t < 432 + 27) __builtin_amdgcn_global_load_lds((const AS1 void*)(wo + (size_t)blockIdx.x * 1728 + (t - 432) * 64), junk, 4, 0, 0);
    }
    f32x4 acc[4][6];
    gemm_core<4, 2, 4, 6, 3, 9, 0, 2>(xn + (size_t)row0 * D, D, w1 + col0, F, smem, acc);
    int tid = threadIdx.x; asm volatile("" : "+v"(tid));
    const int lane = tid & 63, wid = tid >> 6, fr = lane & 15, g = lane >> 4;
    const int wm = wid >> 1, wn = wid & 1;
#pragma unroll
    for (int mb = 0; mb < 4; ++mb) {
      const int m = row0 + wm * 64 + mb * 16 + fr;
#pragma unroll
      for (int nb = 0; nb < 6; ++nb) {
        const int n = col0 + wn * 96 + nb * 16 + 4 * g;
        f32x4 v = acc[mb][nb];
        for (int r = 0; r < 4; ++r) v[r] = silu_f(bfr(v[r]));
        u32x2 w = {pack2(v[0], v[1]), pack2(v[2], v[3])};
        *(u32x2*)(h + (size_t)m * F + n) = w;
      }
    }
  } else {
    const int b = blockIdx.x - 192;
    const int tm = b >> 2, tn = b & 3;
    const int row0 = tm * 128;
    f32x4 acc[4][7];
    gemm_core<2, 4, 4, 7, 7, 9, 1, 7>(xn + (size_t)row0 * D, D, wqkv + tn * 144, NQKV, smem, acc);
    int tid = threadIdx.x; asm volatile("" : "+v"(tid));
    const int lane = tid & 63, wid = tid >> 6, fr = lane & 15, g = lane >> 4;
    const int wm = wid >> 2, wn = wid & 3;
    constexpr int TP = 296;
    u16* T = (u16*)smem;
    u16* PH = (u16*)(smem + 75776);
    u16* VV = (u16*)(smem + 75776 + 49152);
    u32x4 wfr[2][2];
    load_wa(wA, tn * 8 + (wid >> 1), lane, wfr[0][0], wfr[0][1]);
    load_wa(wA, tn * 8 + 4 + (wid >> 1), lane, wfr[1][0], wfr[1][1]);
    LBAR();
#pragma unroll
    for (int mb = 0; mb < 4; ++mb) {
      const int ml = wm * 64 + mb * 16 + fr;
#pragma unroll
      for (int nb = 0; nb < 7; ++nb) {
        const int cb = nb * 4 + wn;
        if (cb < 27) {
          f32x4 v = acc[mb][nb];
          u32x2 w = {pack2(v[0], v[1]), pack2(v[2], v[3])};
          if (cb < 9) *(u32x2*)(qbuf + (size_t)(row0 + ml) * D + tn * 144 + cb * 16 + 4 * g) = w;
          else *(u32x2*)(T + ml * TP + (cb - 9) * 16 + 4 * g) = w;
        }
      }
    }
#pragma unroll 1
    for (int hh = 0; hh < 2; ++hh) {
      LBAR();
#pragma unroll
      for (int u = 0; u < 4; ++u) {
        const int unit = wid * 4 + u, hl = unit >> 3, rb = unit & 7;
        const int head = hh * 4 + hl;
        const u32x4 wa0 = hh ? wfr[1][0] : wfr[0][0], wa1 = hh ? wfr[1][1] : wfr[0][1];
        const unsigned* kr = (const unsigned*)(T + (rb * 16 + fr) * TP + head * DH + 8 * g);
        const u32x4 xb = {kr[0], kr[1], kr[2], kr[3]};
        float e[8], ex[4];
        feat_mfma(xb, wa0, wa1, g == 0, e, ex);
        u16* pr = PH + (hl * 128 + rb * 16 + fr) * 48;
        *(u32x2*)(pr + 4 * g) = (u32x2){pack2(e[0], e[1]), pack2(e[2], e[3])};
        *(u32x2*)(pr + 16 + 4 * g) = (u32x2){pack2(e[4], e[5]), pack2(e[6], e[7])};
        if (g == 0) *(u32x2*)(pr + 32) = (u32x2){pack2(ex[0], ex[1]), pack2(ex[2], ex[3])};
        else *(u32x2*)(pr + 32 + 4 * g) = (u32x2){0u, 0u};
        const unsigned* vr = (const unsigned*)(T + (rb * 16 + fr) * TP + 144 + head * DH);
        u32x4 vv;
        if (g < 2) vv = (u32x4){vr[4 * g], vr[4 * g + 1], vr[4 * g + 2], vr[4 * g + 3]};
        else if (g == 2) vv = (u32x4){vr[8], 0x3f80u, 0u, 0u};
        else vv = (u32x4){0u, 0u, 0u, 0u};
        *(u32x4*)(VV + (hl * 128 + rb * 16 + fr) * 32 + 8 * g) = vv;
      }
      LBAR();
      if (wid < 4) {
        const int hl = wid;
        f32x4 a2[3][2];
#pragma unroll
        for (int i = 0; i < 3; ++i)
#pragma unroll
          for (int j = 0; j < 2; ++j) a2[i][j] = (f32x4){0.f, 0.f, 0.f, 0.f};
        const int q4 = (lane & 15) >> 2, p4 = lane & 3;
        const unsigned sb = (unsigned)(size_t)(AS3 char*)smem;
#pragma unroll
        for (int ks = 0; ks < 4; ++ks) {
          const int r0 = ks * 32 + 8 * g + q4;
          u32x2 pa[3][2], vb[2][2];
#pragma unroll
          for (int t = 0; t < 2; ++t) {
#pragma unroll
            for (int i = 0; i < 3; ++i) rdtr(pa[i][t], sb + 75776 + ((hl * 128 + r0 + 4 * t) * 48 + i * 16 + 4 * p4) * 2, 0);
#pragma unroll
            for (int j = 0; j < 2; ++j) rdtr(vb[j][t], sb + 75776 + 49152 + ((hl * 128 + r0 + 4 * t) * 32 + j * 16 + 4 * p4) * 2, 0);
          }
          asm volatile("s_waitcnt lgkmcnt(0)" ::: "memory");
#pragma unroll
          for (int i = 0; i < 3; ++i) { asm volatile("" : "+v"(pa[i][0]), "+v"(pa[i][1])); }
#pragma unroll
          for (int j = 0; j < 2; ++j) { asm volatile("" : "+v"(vb[j][0]), "+v"(vb[j][1])); }
#pragma unroll
          for (int i = 0; i < 3; ++i)
#pragma unroll
            for (int j = 0; j < 2; ++j) {
              u32x4 aw = {pa[i][0][0], pa[i][0][1], pa[i][1][0], pa[i][1][1]};
              u32x4 bw = {vb[j][0][0], vb[j][0][1], vb[j][1][0], vb[j][1][1]};
              a2[i][j] = __builtin_amdgcn_mfma_f32_16x16x32_bf16(__builtin_bit_cast(bf16x8, aw), __builtin_bit_cast(bf16x8, bw), a2[i][j], 0, 0, 0);
            }
        }
        float* kvp = kvpart + ((size_t)tm * H + tn * 8 + hh * 4 + hl) * (48 * 32);
#pragma unroll
        for (int i = 0; i < 3; ++i)
#pragma unroll
          for (int j = 0; j < 2; ++j)
#pragma unroll
            for (int r = 0; r < 4; ++r) kvp[(i * 16 + 4 * g + r) * 32 + j * 16 + fr] = a2[i][j][r];
      }
    }
  }
}

__device__ __forceinline__ void o_step(const u16* __restrict__ qbuf, const u16* __restrict__ wA, const float* __restrict__ kvpart, u16* __restrict__ o,
                                       int h, int rq, char* smem) {
  const int tid = threadIdx.x, lane = tid & 63, wid = tid >> 6, fr = lane & 15, g = lane >> 4;
  float* kvs = (float*)smem;
  for (int i = tid; i < 48 * 32; i += 512) {
    float a = 0.f;
#pragma unroll
    for (int t = 0; t < 16; ++t) a += kvpart[((size_t)t * H + h) * (48 * 32) + i];
    kvs[i] = bfr(a);
  }
  __syncthreads();
  u32x4 ka[2][2];
#pragma unroll
  for (int j = 0; j < 2; ++j) {
    const int d = fr + 16 * j;
#pragma unroll
    for (int e = 0; e < 4; ++e) {
      ka[0][j][e] = pack2(kvs[(e < 2 ? 4 * g + 2 * e : 16 + 4 * g + 2 * (e - 2)) * 32 + d], kvs[(e < 2 ? 4 * g + 2 * e + 1 : 16 + 4 * g + 2 * (e - 2) + 1) * 32 + d]);
      ka[1][j][e] = (g == 0 && e < 2) ? pack2(kvs[(32 + 2 * e) * 32 + d], kvs[(33 + 2 * e) * 32 + d]) : 0u;
    }
  }
  u32x4 wa0, wa1;
  load_wa(wA, h, lane, wa0, wa1);
  u32x4 xq[8];
#pragma unroll
  for (int u = 0; u < 8; ++u) {
    const int row = rq * 1024 + wid * 128 + u * 16 + fr;
    const unsigned* qr = (const unsigned*)(qbuf + (size_t)row * D + h * DH + 8 * g);
    if (h == H - 1 && g >= 2) xq[u] = (g == 2) ? (u32x4){qr[0], 0u, 0u, 0u} : (u32x4){0u, 0u, 0u, 0u};
    else xq[u] = (u32x4){qr[0], qr[1], qr[2], qr[3]};
  }
#pragma unroll
  for (int u = 0; u < 8; ++u) {
    const int row = rq * 1024 + wid * 128 + u * 16 + fr;
    const u32x4 xb = xq[u];
    float e[8], ex[4];
    feat_mfma(xb, wa0, wa1, g == 0, e, ex);
    const u32x4 pb0 = {pack2(e[0], e[1]), pack2(e[2], e[3]), pack2(e[4], e[5]), pack2(e[6], e[7])};
    const u32x4 pb1 = {pack2(ex[0], ex[1]), pack2(ex[2], ex[3]), 0u, 0u};
    f32x4 n[2];
#pragma unroll
    for (int j = 0; j < 2; ++j) {
      f32x4 z = {0.f, 0.f, 0.f, 0.f};
      z = __builtin_amdgcn_mfma_f32_16x16x32_bf16(__builtin_bit_cast(bf16x8, ka[0][j]), __builtin_bit_cast(bf16x8, pb0), z, 0, 0, 0);
      n[j] = __builtin_amdgcn_mfma_f32_16x16x32_bf16(__builtin_bit_cast(bf16x8, ka[1][j]), __builtin_bit_cast(bf16x8, pb1), z, 0, 0, 0);
    }
    float den = g == 0 ? n[1][2] : 0.f;
    den = red16_32_add(den);
    den = bfr(bfr(den) + 1e-6f);
    const float rden = __builtin_amdgcn_rcpf(den);
    unsigned* orow = (unsigned*)(o + (size_t)row * D + h * DH);
    orow[2 * g] = pack2(bfr(n[0][0]) * rden, bfr(n[0][1]) * rden);
    orow[2 * g + 1] = pack2(bfr(n[0][2]) * rden, bfr(n[0][3]) * rden);
    if (g == 0) orow[8] = pack2(bfr(n[1][0]) * rden, bfr(n[1][1]) * rden);
  }
}

__global__ __launch_bounds__(512) void gemm2_kernel(const u16* __restrict__ h, const u16* __restrict__ w2, float* __restrict__ part,
                                                    const u16* __restrict__ qbuf, const u16* __restrict__ wA, const float* __restrict__ kvpart,
                                                    u16* __restrict__ o) {
  extern __shared__ __attribute__((aligned(16))) char smem[];
  const int tid = threadIdx.x, lane = tid & 63, wid = tid >> 6;
  if (blockIdx.x >= 192) {
    const int b = blockIdx.x - 192;
    o_step(qbuf, wA, kvpart, o, b >> 1, b & 1, smem);
    return;
  }
  const int fr = lane & 15, g = lane >> 4;
  const int split = blockIdx.x & 3, tile = blockIdx.x >> 2;
  const int tm = tile / 3, tn = tile % 3;
  const int row0 = tm * 128, col0 = tn * 192, k0 = split * 1152;
  f32x4 acc[2][6];
  gemm_core<4, 2, 2, 6, 3, 18, 0>(h + (size_t)row0 * F + k0, F, w2 + (size_t)k0 * D + col0, D, smem, acc);
  const int wm = wid >> 1, wn = wid & 1;
  u16* P = (u16*)part + (size_t)split * S * D;
#pragma unroll
  for (int mb = 0; mb < 2; ++mb) {
    const int m = row0 + wm * 32 + mb * 16 + fr;
#pragma unroll
    for (int nb = 0; nb < 6; ++nb) {
      const int n = col0 + wn * 96 + nb * 16 + 4 * g;
      *(u32x2*)(P + (size_t)m * D + n) = (u32x2){pack2(acc[mb][nb][0], acc[mb][nb][1]), pack2(acc[mb][nb][2], acc[mb][nb][3])};
    }
  }
}

__global__ __launch_bounds__(256) void final_kernel(const u16* __restrict__ x, const u16* __restrict__ o, const u16* __restrict__ wo,
                                                    const float* __restrict__ part, u16* __restrict__ out, int nsplit) {
  extern __shared__ __attribute__((aligned(16))) char smem[];
  const int tid = threadIdx.x, lane = tid & 63, wid = tid >> 6;
  const int fr = lane & 15, g = lane >> 4;
  const int tm = blockIdx.x / 9, tn = blockIdx.x % 9;
  const int row0 = tm * 64, col0 = tn * 64;
  const int wm = wid >> 1, wn = wid & 1;
  float res[2][2][4];
#pragma unroll
  for (int mb = 0; mb < 2; ++mb)
#pragma unroll
    for (int nb = 0; nb < 2; ++nb) {
      const int m = row0 + wm * 32 + mb * 16 + fr, n = col0 + wn * 32 + nb * 16 + 4 * g;
      f32x4 a = {0.f, 0.f, 0.f, 0.f};
#pragma unroll
      for (int sp = 0; sp < 4; ++sp) { const u32x2 p = *(const u32x2*)((const u16*)part + (size_t)sp * S * D + (size_t)m * D + n);
        a += (f32x4){__uint_as_float(p[0] << 16), __uint_as_float(p[0] & 0xffff0000u), __uint_as_float(p[1] << 16), __uint_as_float(p[1] & 0xffff0000u)}; }
      for (int r = 0; r < 4; ++r) res[mb][nb][r] = a[r];
    }
#pragma unroll
  for (int mb = 0; mb < 2; ++mb)
#pragma unroll
    for (int nb = 0; nb < 2; ++nb)
      for (int r = 0; r < 4; ++r) asm volatile("" : "+v"(res[mb][nb][r]));
  f32x4 acc[2][2];
  gemm_core<2, 2, 2, 2, 1, 9, 0>(o + (size_t)row0 * D, D, wo + col0, D, smem, acc);
#pragma unroll
  for (int mb = 0; mb < 2; ++mb)
#pragma unroll
    for (int nb = 0; nb < 2; ++nb) {
      const int m = row0 + wm * 32 + mb * 16 + fr, n = col0 + wn * 32 + nb * 16 + 4 * g;
      u32x2 xv = *(const u32x2*)(x + (size_t)m * D + n);
      float xs[4] = {__uint_as_float(xv[0] << 16), __uint_as_float(xv[0] & 0xffff0000u), __uint_as_float(xv[1] << 16), __uint_as_float(xv[1] & 0xffff0000u)};
      float v[4];
      for (int r = 0; r < 4; ++r) v[r] = bfr(xs[r] + bfr(acc[mb][nb][r])) + bfr(res[mb][nb][r]);
      u32x2 w = {pack2(v[0], v[1]), pack2(v[2], v[3])};
      *(u32x2*)(out + (size_t)m * D + n) = w;
    }
}

extern "C" void kernel_launch(void* const* d_in, const int* in_sizes, int n_in, void* d_out, int out_size, void* d_ws, size_t ws_size,
                              hipStream_t stream) {
  const u16* x = (const u16*)d_in[0];
  const u16* wqkv = (const u16*)d_in[1];
  const u16* wphi = (const u16*)d_in[2];
  const u16* wo = (const u16*)d_in[3];
  const u16* w1 = (const u16*)d_in[5];
  const u16* w2 = (const u16*)d_in[6];
  u16* out = (u16*)d_out;
  char* ws = (char*)d_ws;
  size_t off = 0;
  auto take = [&](size_t bytes) { char* p = ws + off; off += (bytes + 255) & ~(size_t)255; return p; };
  u16* qbuf = (u16*)take((size_t)S * D * 2);
  u16* h = (u16*)take((size_t)S * F * 2);
  u16* o = (u16*)take((size_t)S * D * 2);
  float* kvpart = (float*)take((size_t)16 * H * 48 * 32 * 4);
  const u16* wA = wphi;
  const u16* xn = x;
  constexpr int NSPLIT = 4;
  float* part = (float*)take((size_t)NSPLIT * S * D * 4);

  const int shm1 = 160 * 1024, shm2 = (3 * 128 + 2 * 192) * 128;
  hipFuncSetAttribute((const void*)gemm1_kernel, hipFuncAttributeMaxDynamicSharedMemorySize, shm1);
  hipFuncSetAttribute((const void*)gemm2_kernel, hipFuncAttributeMaxDynamicSharedMemorySize, shm2);
  gemm1_kernel<<<256, 512, shm1, stream>>>(xn, w1, wqkv, wA, h, qbuf, kvpart, w2, wo);
  gemm2_kernel<<<192 + 64, 512, shm2, stream>>>(h, w2, part, qbuf, wA, kvpart, o);
  final_kernel<<<32 * 9, 256, (3 * 64 + 2 * 64) * 128, stream>>>(x, o, wo, part, out, NSPLIT);
}
```

```cpp
#include <hip/hip_runtime.h>
#include <hip/hip_bf16.h>

typedef unsigned short u16;
typedef __attribute__((ext_vector_type(8))) __bf16 bf16x8;
typedef __attribute__((ext_vector_type(4))) float f32x4;
typedef __attribute__((ext_vector_type(4))) unsigned u32x4;
typedef __attribute__((ext_vector_type(2))) unsigned u32x2;
#define AS1 __attribute__((address_space(1)))
#define AS3 __attribute__((address_space(3)))
typedef __attribute__((ext_vector_type(2))) float f32x2;
typedef const __attribute__((address_space(4))) float* cfp;

constexpr int S = 2048, D = 576, H = 32, DH = 18, F = 4608;
constexpr int NQKV = 3 * D;
constexpr int NFEAT = 2 * DH;
constexpr int KVSZ = NFEAT * DH + NFEAT;

__device__ __forceinline__ float bf2f(u16 v) { return __uint_as_float(((unsigned)v) << 16); }
__device__ __forceinline__ u16 f2bf(float f) { __bf16 b = (__bf16)f; return __builtin_bit_cast(u16, b); }
__device__ __forceinline__ unsigned pack2(float lo, float hi) { return (unsigned)f2bf(lo) | ((unsigned)f2bf(hi) << 16); }
__device__ __forceinline__ float bfr(float f) { return bf2f(f2bf(f)); }
__device__ __forceinline__ float silu_f(float v) { return v * __builtin_amdgcn_rcpf(1.f + __builtin_amdgcn_exp2f(-1.4426950408889634f * v)); }

#define SBAR() __builtin_amdgcn_sched_barrier(0)
#define LBAR() do { asm volatile("s_waitcnt lgkmcnt(0)" ::: "memory"); __builtin_amdgcn_s_barrier(); asm volatile("" ::: "memory"); } while (0)
template <int N> __device__ __forceinline__ void waitv() { asm volatile("s_waitcnt vmcnt(%0)" ::"n"(N) : "memory"); }
__device__ __forceinline__ void rd128(u32x4& d, unsigned addr, int off) { asm volatile("ds_read_b128 %0, %1 offset:%2" : "=v"(d) : "v"(addr), "i"(off)); }
__device__ __forceinline__ void rdtr(u32x2& d, unsigned addr, int off) { asm volatile("ds_read_b64_tr_b16 %0, %1 offset:%2" : "=v"(d) : "v"(addr), "i"(off)); }

typedef __attribute__((ext_vector_type(2))) __bf16 bf16x2;
__device__ __forceinline__ float dot2(unsigned a, unsigned b, float c) { return __builtin_amdgcn_fdot2_f32_bf16(__builtin_bit_cast(bf16x2, a), __builtin_bit_cast(bf16x2, b), c, false); }
__device__ __forceinline__ void load_wa(const u16* __restrict__ wphi, int head, int lane, u32x4& wa0, u32x4& wa1) {
  const int fr = lane & 15, g = lane >> 4;
  const u16* w = wphi + head * (DH * DH) + fr;
  unsigned a[8], b[8];
#pragma unroll
  for (int j = 0; j < 8; ++j) {
    const int d = 8 * g + j;
    a[j] = d < DH ? w[d * DH] : 0u;
    b[j] = (d < DH && fr < 2) ? w[d * DH + 16] : 0u;
  }
#pragma unroll
  for (int e = 0; e < 4; ++e) { wa0[e] = a[2 * e] | (a[2 * e + 1] << 16); wa1[e] = b[2 * e] | (b[2 * e + 1] << 16); }
}

__device__ __forceinline__ float red16_32_max(float v) {
  auto r = __builtin_amdgcn_permlane16_swap(__float_as_uint(v), __float_as_uint(v), false, false);
  v = fmaxf(__uint_as_float(r[0]), __uint_as_float(r[1]));
  r = __builtin_amdgcn_permlane32_swap(__float_as_uint(v), __float_as_uint(v), false, false);
  return fmaxf(__uint_as_float(r[0]), __uint_as_float(r[1]));
}
__device__ __forceinline__ float red16_32_add(float v) {
  auto r = __builtin_amdgcn_permlane16_swap(__float_as_uint(v), __float_as_uint(v), false, false);
  v = __uint_as_float(r[0]) + __uint_as_float(r[1]);
  r = __builtin_amdgcn_permlane32_swap(__float_as_uint(v), __float_as_uint(v), false, false);
  return __uint_as_float(r[0]) + __uint_as_float(r[1]);
}
template <int WM, int WN, int MB, int NB, int BSUB, int NK, int QKVMAP, int LNF = 0>
__device__ __forceinline__ void gemm_core(const u16* __restrict__ A, int lda, const u16* __restrict__ B, int ldb, char* smem, f32x4 (&acc)[MB][NB]) {
  constexpr int BM = WM * MB * 16, NW = WM * WN;
#define CBMAP(wn_, nb_) (QKVMAP ? (nb_) * WN + (wn_) : (wn_) * NB + (nb_))
  constexpr int ABYTES = BM * 128, BBYTES = BSUB * 8192;
  constexpr int BOFF = 3 * ABYTES;
  constexpr int APW = BM / 8 / NW, BPW = BSUB * 8 / NW;
  static_assert((BM / 8) % NW == 0 && (BSUB * 8) % NW == 0, "pieces");
  int tid = threadIdx.x; asm volatile("" : "+v"(tid));
  const int lane = tid & 63, wid = __builtin_amdgcn_readfirstlane(tid >> 6);
  const int wm = wid / WN, wn = wid % WN;
  unsigned aoffs[APW], boffs[BPW];
  {
    const int r8 = lane >> 3, c = lane & 7;
#pragma unroll
    for (int p = 0; p < APW; ++p) {
      const int piece = p * NW + wid;
      aoffs[p] = (unsigned)((piece * 8 + r8) * lda + ((c ^ r8) << 3)) * 2u;
    }
#pragma unroll
    for (int p = 0; p < BPW; ++p) {
      const int pb = p * NW + wid, sub = pb >> 3, j = pb & 7;
      const int r = j * 8 + r8;
      const int swz = (((r >> 1) & 1) << 1) | ((r >> 3) & 1);
      const int chunk = (((c >> 1) ^ swz) << 1) | (c & 1);
      int col = sub * 64 + (chunk << 3);
      if (QKVMAP) {
        const int c8 = chunk << 3;
        col = c8 + (p == 2 && c8 >= 16 ? 432 : 0) + (p == 4 && c8 >= 32 ? 432 : 0) - (p == 6 && c8 >= 48 ? 16 : 0);
      }
      boffs[p] = (unsigned)(r * ldb + col) * 2u;
    }
  }
  auto stageA1 = [&](int slot, int p, int kt) {
    __builtin_amdgcn_global_load_lds((const AS1 void*)((const char*)(A + kt * 64 + (size_t)p * NW * 8 * lda) + aoffs[0]), (AS3 void*)(smem + slot * ABYTES + (p * NW + wid) * 1024), 16, 0, 0);
  };
  auto stageB1 = [&](int slot, int p, int kt) {
    __builtin_amdgcn_global_load_lds((const AS1 void*)((const char*)(B + (size_t)kt * 64 * ldb + (QKVMAP ? p * 64 + (p > 4 ? 864 : p > 2 ? 432 : 0) : ((p * NW) >> 3) * 64 + ((p * NW) & 7) * 8 * ldb)) + (QKVMAP ? boffs[p & 1 ? 0 : p] : boffs[0])), (AS3 void*)(smem + BOFF + slot * BBYTES + (p * NW + wid) * 1024), 16, 0, 0);
  };
#pragma unroll
  for (int p = 0; p < BPW; ++p) stageB1(0, p, 0);
#pragma unroll
  for (int p = 0; p < APW; ++p) stageA1(0, p, 0);
#pragma unroll
  for (int p = 0; p < APW; ++p) stageA1(1, p, 1);

  const int fr = lane & 15, g = lane >> 4, q = (lane & 15) >> 2, p4 = lane & 3;
  const unsigned sbase = (unsigned)(size_t)(AS3 char*)smem;
  constexpr bool AIMM = (2 * ABYTES + (MB - 1) * 2048) < 65536;
  unsigned aaddr[2][AIMM ? 1 : 3];
  {
    const int r = wm * MB * 16 + fr;
#pragma unroll
    for (int s = 0; s < (AIMM ? 1 : 3); ++s) {
      aaddr[0][s] = sbase + s * ABYTES + r * 128 + (((0 + g) ^ (r & 7)) << 4);
      aaddr[1][s] = sbase + s * ABYTES + r * 128 + (((4 + g) ^ (r & 7)) << 4);
    }
  }
  unsigned baddr[NB];
  {
    const int sB = (((q >> 1) & 1) << 1) | (g & 1);
    const int kr = 8 * g + q;
#pragma unroll
    for (int nb = 0; nb < NB; ++nb) {
      const int cb = CBMAP(wn, nb);
      baddr[nb] = sbase + BOFF + (QKVMAP ? nb * BBYTES : (cb >> 2) * 8192) + kr * 128 + ((((cb & 3) ^ sB)) << 5) + ((p4 >> 1) << 4) + 8 * (p4 & 1);
    }
  }
#pragma unroll
  for (int i = 0; i < MB; ++i)
#pragma unroll
    for (int j = 0; j < NB; ++j) acc[i][j] = (f32x4){0.f, 0.f, 0.f, 0.f};
  u32x4 af[2][MB];
  u32x2 bt[2][NB][2];
  constexpr int RPW = MB / WN > 0 ? MB / WN : 1, NBG = (NB + WM - 1) / WM;
  float rsm[RPW], rq[RPW], cs_[NBG];
#pragma unroll
  for (int i = 0; i < RPW; ++i) rsm[i] = rq[i] = 0.f;
#pragma unroll
  for (int j = 0; j < NBG; ++j) cs_[j] = 0.f;
  auto wait_set = [&](int set) {
#pragma unroll
    for (int i = 0; i < MB; ++i) asm volatile("" : "+v"(af[set][i]));
    asm volatile("s_waitcnt lgkmcnt(0)" ::: "memory");
#pragma unroll
    for (int i = 0; i < MB; ++i) asm volatile("" : "+v"(af[set][i]));
#pragma unroll
    for (int j = 0; j < NB; ++j) asm volatile("" : "+v"(bt[set][j][0]), "+v"(bt[set][j][1]));
  };
  constexpr int NRD = 2 * NB + MB;
  constexpr int NRS = NRD < MB * NB ? NRD : MB * NB;
#define PHASE(cs, rs, aslot, bslot, kk, do_reads, sb, sa, kb, ka)                                                                            \
  _Pragma("unroll") for (int i = 0; i < MB * NB; ++i) {                                                                              \
    const int mb = i / NB, nb = i % NB;                                                                                              \
    u32x4 bw = {bt[cs][nb][0][0], bt[cs][nb][0][1], bt[cs][nb][1][0], bt[cs][nb][1][1]};                                            \
    acc[mb][nb] = __builtin_amdgcn_mfma_f32_16x16x32_bf16(__builtin_bit_cast(bf16x8, bw), __builtin_bit_cast(bf16x8, af[cs][mb]), acc[mb][nb], 0, 0, 0); \
    SBAR();                                                                                                                          \
    if (LNF && i == 1) {                                                                                                             \
      if (LNF & 1) {                                                                             \
        _Pragma("unroll") for (int ii = 0; ii < RPW; ++ii) {                                                                         \
          u32x4 f = af[cs][ii];                                                                                                      \
          _Pragma("unroll") for (int w = 1; w < WN; ++w) f = (wn == w) ? af[cs][w * RPW + ii] : f;                                   \
          _Pragma("unroll") for (int e = 0; e < 4; ++e) { rsm[ii] = dot2(f[e], 0x3f803f80u, rsm[ii]); rq[ii] = dot2(f[e], f[e], rq[ii]); } \
        }                                                                                                                            \
      } else {                                                                                                                       \
        _Pragma("unroll") for (int w = 0; w < WN; ++w) if (wn == w) {                                                                \
          _Pragma("unroll") for (int ii = 0; ii < RPW; ++ii) _Pragma("unroll") for (int e = 0; e < 4; ++e) {                         \
            rsm[ii] = dot2(af[cs][w * RPW + ii][e], 0x3f803f80u, rsm[ii]); rq[ii] = dot2(af[cs][w * RPW + ii][e], af[cs][w * RPW + ii][e], rq[ii]); } \
        }                                                                                                                            \
      }                                                                                                                              \
      if (LNF & 4) {                                                                                                                 \
        _Pragma("unroll") for (int jj = 0; jj < NBG; ++jj) {                                                                         \
          u32x2 f0 = bt[cs][jj][0], f1 = bt[cs][jj][1];                                                                              \
          _Pragma("unroll") for (int w = 1; w < WM; ++w) { const int nbw = w * NBG + jj < NB ? w * NBG + jj : jj;                    \
            f0 = (wm == w) ? bt[cs][nbw][0] : f0; f1 = (wm == w) ? bt[cs][nbw][1] : f1; }                                            \
          cs_[jj] = dot2(f0[0], 0x3f803f80u, dot2(f0[1], 0x3f803f80u, dot2(f1[0], 0x3f803f80u, dot2(f1[1], 0x3f803f80u, cs_[jj])))); \
        }                                                                                                                            \
      } else {                                                                                                                       \
        _Pragma("unroll") for (int w = 0; w < WM; ++w) if (wm == w) {                                                                \
          _Pragma("unroll") for (int jj = 0; jj < NBG; ++jj) if (w * NBG + jj < NB) _Pragma("unroll") for (int e = 0; e < 4; ++e)    \
            cs_[jj] = dot2(bt[cs][w * NBG + jj][e >> 1][e & 1], 0x3f803f80u, cs_[jj]);                                               \
        }                                                                                                                            \
      }                                                                                                                              \
      SBAR();                                                                                                                        \
    }                                                                                                                                \
    if (do_reads) {                                                                                                                  \
      _Pragma("unroll") for (int r = (i < NRS ? (i * NRD) / NRS : NRD); r < (i < NRS ? ((i + 1) * NRD) / NRS : NRD); ++r) {          \
        if (r < 2 * NB) rdtr(bt[rs][r >> 1][r & 1], QKVMAP ? baddr[bslot] : baddr[r >> 1], (QKVMAP ? (r >> 1) * 8192 : (bslot) * BBYTES) + (kk) * 4096 + (r & 1) * 512);                  \
        else rd128(af[rs][r - 2 * NB], aaddr[kk][AIMM ? 0 : (aslot)], (AIMM ? (aslot) * ABYTES : 0) + (r - 2 * NB) * 2048);                                                       \
        SBAR();                                                                                                                      \
      }                                                                                                                              \
    }                                                                                                                                \
    _Pragma("unroll") for (int s = (i * (APW + BPW)) / (MB * NB); s < ((i + 1) * (APW + BPW)) / (MB * NB); ++s) {                  \
      if (s < BPW) { if ((sb) >= 0) stageB1(sb, s, kb); }                                                                                 \
      else { if ((sa) >= 0) stageA1(sa, s - BPW, ka); }                                                                                 \
      SBAR();                                                                                                                        \
    }                                                                                                                                \
  }
  waitv<APW>();
  __builtin_amdgcn_s_barrier();
#pragma unroll
  for (int nb = 0; nb < NB; ++nb) { rdtr(bt[0][nb][0], baddr[QKVMAP ? 0 : nb], QKVMAP ? nb * 8192 : 0); rdtr(bt[0][nb][1], baddr[QKVMAP ? 0 : nb], (QKVMAP ? nb * 8192 : 0) + 512); }
#pragma unroll
  for (int mb = 0; mb < MB; ++mb) rd128(af[0][mb], aaddr[0][0], mb * 2048);
#pragma unroll
  for (int it = 0; it < NK; ++it) {
    wait_set(0);
    SBAR();
    PHASE(0, 1, it % 3, it & 1, 1, true, it == 0 ? 1 : -1, it == 0 ? 2 : -1, 1, 2);
    wait_set(1);
    if (it + 2 < NK) waitv<APW>(); else waitv<0>();
    __builtin_amdgcn_s_barrier();
    SBAR();
    PHASE(1, 0, (it + 1) % 3, (it + 1) & 1, 0, it + 1 < NK, (it + 2 < NK) ? (it & 1) : -1, (it + 3 < NK) ? (it % 3) : -1, it + 2, it + 3);
  }
#undef PHASE
  if (LNF) {
    constexpr int BN = WN * NB * 16;
    float* st = (float*)smem;
#pragma unroll
    for (int i = 0; i < RPW; ++i) { rsm[i] = red16_32_add(rsm[i]); rq[i] = red16_32_add(rq[i]); }
#pragma unroll
    for (int j = 0; j < NBG; ++j) cs_[j] = red16_32_add(cs_[j]);
    __syncthreads();
    if (g == 0) {
#pragma unroll
      for (int i = 0; i < RPW; ++i) { const int r = (wm * MB + wn * RPW + i) * 16 + fr; st[r] = rsm[i]; st[BM + r] = rq[i]; }
#pragma unroll
      for (int j = 0; j < NBG; ++j) if (wm * NBG + j < NB) st[2 * BM + CBMAP(wn, wm * NBG + j) * 16 + fr] = cs_[j];
    }
    __syncthreads();
    float mean[MB], rstd[MB];
#pragma unroll
    for (int mb = 0; mb < MB; ++mb) {
      const int r = (wm * MB + mb) * 16 + fr;
      mean[mb] = st[r] * (1.f / D);
      rstd[mb] = rsqrtf(fmaxf(st[BM + r] * (1.f / D) - mean[mb] * mean[mb], 0.f) + 1e-5f);
    }
#pragma unroll
    for (int nb = 0; nb < NB; ++nb) {
      const f32x4 c4 = *(const f32x4*)(st + 2 * BM + CBMAP(wn, nb) * 16 + 4 * g);
#pragma unroll
      for (int mb = 0; mb < MB; ++mb)
#pragma unroll
        for (int e = 0; e < 4; ++e) acc[mb][nb][e] = rstd[mb] * (acc[mb][nb][e] - mean[mb] * c4[e]);
    }
  }
}

__device__ __forceinline__ void feat_mfma(u32x4 xb, u32x4 wa0, u32x4 wa1, bool g0, float (&e)[8], float (&ex)[4]) {
  f32x4 z = {0.f, 0.f, 0.f, 0.f};
  f32x4 p0 = __builtin_amdgcn_mfma_f32_16x16x32_bf16(__builtin_bit_cast(bf16x8, wa0), __builtin_bit_cast(bf16x8, xb), z, 0, 0, 0);
  f32x4 p1 = __builtin_amdgcn_mfma_f32_16x16x32_bf16(__builtin_bit_cast(bf16x8, wa1), __builtin_bit_cast(bf16x8, xb), z, 0, 0, 0);
  float p[4], q[2];
#pragma unroll
  for (int r = 0; r < 4; ++r) p[r] = bfr(p0[r]);
  q[0] = bfr(p1[0]); q[1] = bfr(p1[1]);
  float mx = fmaxf(fmaxf(p[0], p[1]), fmaxf(p[2], p[3])), mn = fminf(fminf(p[0], p[1]), fminf(p[2], p[3]));
  if (g0) { mx = fmaxf(mx, fmaxf(q[0], q[1])); mn = fminf(mn, fminf(q[0], q[1])); }
  mx = red16_32_max(mx);
  mn = -red16_32_max(-mn);
  float s1 = 0.f, s2 = 0.f;
#pragma unroll
  for (int r = 0; r < 4; ++r) { e[r] = __expf(p[r] - mx); e[4 + r] = __expf(mn - p[r]); s1 += e[r]; s2 += e[4 + r]; }
#pragma unroll
  for (int r = 0; r < 2; ++r) { ex[r] = g0 ? __expf(q[r] - mx) : 0.f; ex[2 + r] = g0 ? __expf(mn - q[r]) : 0.f; s1 += ex[r]; s2 += ex[2 + r]; }
  s1 = red16_32_add(s1);
  s2 = red16_32_add(s2);
  const float r1 = __builtin_amdgcn_rcpf(s1), r2 = __builtin_amdgcn_rcpf(s2);
#pragma unroll
  for (int r = 0; r < 4; ++r) { e[r] = e[r] * r1; e[4 + r] = e[4 + r] * r2; }
  ex[0] = ex[0] * r1; ex[1] = ex[1] * r1; ex[2] = ex[2] * r2; ex[3] = ex[3] * r2;
}

__global__ __launch_bounds__(512) void gemm1_kernel(const u16* __restrict__ xn, const u16* __restrict__ w1, const u16* __restrict__ wqkv,
                                                    const u16* __restrict__ wA, u16* __restrict__ h, u16* __restrict__ qbuf, float* __restrict__ kvpart,
                                                    const u16* __restrict__ w2, const u16* __restrict__ wo) {
  extern __shared__ __attribute__((aligned(16))) char smem[];
  if (blockIdx.x < 192) {
    const int tm = blockIdx.x / 24, tn = blockIdx.x % 24;
    const int row0 = tm * 256, col0 = tn * 192;
    {
      const int t = threadIdx.x;
      AS3 void* junk = (AS3 void*)(smem + 147456 + (t >> 6) * 256);
      if (t >= 431) __builtin_amdgcn_global_load_lds((const AS1 void*)(wqkv + (size_t)blockIdx.x * 5184 + (t - 431) * 64), junk, 4, 0, 0);
      else if (t < 216) __builtin_amdgcn_global_load_lds((const AS1 void*)(w1 + (size_t)(tm * 72 + t / 3) * F + col0 + (t % 3) * 64), junk, 4, 0, 0);
      else if (t < 216 + 27) __builtin_amdgcn_global_load_lds((const AS1 void*)(wo + (size_t)blockIdx.x * 1728 + (t - 216) * 64), junk, 4, 0, 0);
    }
    if (threadIdx.x < 216) __builtin_amdgcn_global_load_lds((const AS1 void*)(w2 + (size_t)blockIdx.x * 13824 + threadIdx.x * 64), (AS3 void*)(smem + 147456 + 2048 + (threadIdx.x >> 6) * 256), 4, 0, 0);
    f32x4 acc[4][6];
    gemm_core<4, 2, 4, 6, 3, 9, 0, 2>(xn + (size_t)row0 * D, D, w1 + col0, F, smem, acc);
    int tid = threadIdx.x; asm volatile("" : "+v"(tid));
    const int lane = tid & 63, wid = tid >> 6, fr = lane & 15, g = lane >> 4;
    const int wm = wid >> 1, wn = wid & 1;
#pragma unroll
    for (int mb = 0; mb < 4; ++mb) {
      const int m = row0 + wm * 64 + mb * 16 + fr;
#pragma unroll
      for (int nb = 0; nb < 6; ++nb) {
        const int n = col0 + wn * 96 + nb * 16 + 4 * g;
        f32x4 v = acc[mb][nb];
        for (int r = 0; r < 4; ++r) v[r] = silu_f(bfr(v[r]));
        u32x2 w = {pack2(v[0], v[1]), pack2(v[2], v[3])};
        *(u32x2*)(h + (size_t)m * F + n) = w;
      }
    }
  } else {
    const int b = blockIdx.x - 192;
    const int tm = b >> 2, tn = b & 3;
    const int row0 = tm * 128;
    f32x4 acc[4][7];
    gemm_core<2, 4, 4, 7, 7, 9, 1, 7>(xn + (size_t)row0 * D, D, wqkv + tn * 144, NQKV, smem, acc);
    int tid = threadIdx.x; asm volatile("" : "+v"(tid));
    const int lane = tid & 63, wid = tid >> 6, fr = lane & 15, g = lane >> 4;
    const int wm = wid >> 2, wn = wid & 3;
    constexpr int TP = 296;
    u16* T = (u16*)smem;
    u16* PH = (u16*)(smem + 75776);
    u16* VV = (u16*)(smem + 75776 + 49152);
    u32x4 wfr[2][2];
    load_wa(wA, tn * 8 + (wid >> 1), lane, wfr[0][0], wfr[0][1]);
    load_wa(wA, tn * 8 + 4 + (wid >> 1), lane, wfr[1][0], wfr[1][1]);
    LBAR();
#pragma unroll
    for (int mb = 0; mb < 4; ++mb) {
      const int ml = wm * 64 + mb * 16 + fr;
#pragma unroll
      for (int nb = 0; nb < 7; ++nb) {
        const int cb = nb * 4 + wn;
        if (cb < 27) {
          f32x4 v = acc[mb][nb];
          u32x2 w = {pack2(v[0], v[1]), pack2(v[2], v[3])};
          if (cb < 9) *(u32x2*)(qbuf + (size_t)(row0 + ml) * D + tn * 144 + cb * 16 + 4 * g) = w;
          else *(u32x2*)(T + ml * TP + (cb - 9) * 16 + 4 * g) = w;
        }
      }
    }
#pragma unroll 1
    for (int hh = 0; hh < 2; ++hh) {
      LBAR();
#pragma unroll
      for (int u = 0; u < 4; ++u) {
        const int unit = wid * 4 + u, hl = unit >> 3, rb = unit & 7;
        const int head = hh * 4 + hl;
        const u32x4 wa0 = hh ? wfr[1][0] : wfr[0][0], wa1 = hh ? wfr[1][1] : wfr[0][1];
        const unsigned* kr = (const unsigned*)(T + (rb * 16 + fr) * TP + head * DH + 8 * g);
        const u32x4 xb = {kr[0], kr[1], kr[2], kr[3]};
        float e[8], ex[4];
        feat_mfma(xb, wa0, wa1, g == 0, e, ex);
        u16* pr = PH + (hl * 128 + rb * 16 + fr) * 48;
        *(u32x2*)(pr + 4 * g) = (u32x2){pack2(e[0], e[1]), pack2(e[2], e[3])};
        *(u32x2*)(pr + 16 + 4 * g) = (u32x2){pack2(e[4], e[5]), pack2(e[6], e[7])};
        if (g == 0) *(u32x2*)(pr + 32) = (u32x2){pack2(ex[0], ex[1]), pack2(ex[2], ex[3])};
        else *(u32x2*)(pr + 32 + 4 * g) = (u32x2){0u, 0u};
        const unsigned* vr = (const unsigned*)(T + (rb * 16 + fr) * TP + 144 + head * DH);
        u32x4 vv;
        if (g < 2) vv = (u32x4){vr[4 * g], vr[4 * g + 1], vr[4 * g + 2], vr[4 * g + 3]};
        else if (g == 2) vv = (u32x4){vr[8], 0x3f80u, 0u, 0u};
        else vv = (u32x4){0u, 0u, 0u, 0u};
        *(u32x4*)(VV + (hl * 128 + rb * 16 + fr) * 32 + 8 * g) = vv;
      }
      LBAR();
      if (wid < 4) {
        const int hl = wid;
        f32x4 a2[3][2];
#pragma unroll
        for (int i = 0; i < 3; ++i)
#pragma unroll
          for (int j = 0; j < 2; ++j) a2[i][j] = (f32x4){0.f, 0.f, 0.f, 0.f};
        const int q4 = (lane & 15) >> 2, p4 = lane & 3;
        const unsigned sb = (unsigned)(size_t)(AS3 char*)smem;
#pragma unroll
        for (int ks = 0; ks < 4; ++ks) {
          const int r0 = ks * 32 + 8 * g + q4;
          u32x2 pa[3][2], vb[2][2];
#pragma unroll
          for (int t = 0; t < 2; ++t) {
#pragma unroll
            for (int i = 0; i < 3; ++i) rdtr(pa[i][t], sb + 75776 + ((hl * 128 + r0 + 4 * t) * 48 + i * 16 + 4 * p4) * 2, 0);
#pragma unroll
            for (int j = 0; j < 2; ++j) rdtr(vb[j][t], sb + 75776 + 49152 + ((hl * 128 + r0 + 4 * t) * 32 + j * 16 + 4 * p4) * 2, 0);
          }
          asm volatile("s_waitcnt lgkmcnt(0)" ::: "memory");
#pragma unroll
          for (int i = 0; i < 3; ++i) { asm volatile("" : "+v"(pa[i][0]), "+v"(pa[i][1])); }
#pragma unroll
          for (int j = 0; j < 2; ++j) { asm volatile("" : "+v"(vb[j][0]), "+v"(vb[j][1])); }
#pragma unroll
          for (int i = 0; i < 3; ++i)
#pragma unroll
            for (int j = 0; j < 2; ++j) {
              u32x4 aw = {pa[i][0][0], pa[i][0][1], pa[i][1][0], pa[i][1][1]};
              u32x4 bw = {vb[j][0][0], vb[j][0][1], vb[j][1][0], vb[j][1][1]};
              a2[i][j] = __builtin_amdgcn_mfma_f32_16x16x32_bf16(__builtin_bit_cast(bf16x8, aw), __builtin_bit_cast(bf16x8, bw), a2[i][j], 0, 0, 0);
            }
        }
        float* kvp = kvpart + ((size_t)tm * H + tn * 8 + hh * 4 + hl) * (48 * 32);
#pragma unroll
        for (int i = 0; i < 3; ++i)
#pragma unroll
          for (int j = 0; j < 2; ++j)
#pragma unroll
            for (int r = 0; r < 4; ++r) kvp[(i * 16 + 4 * g + r) * 32 + j * 16 + fr] = a2[i][j][r];
      }
    }
  }
}

__device__ __forceinline__ void o_step(const u16* __restrict__ qbuf, const u16* __restrict__ wA, const float* __restrict__ kvpart, u16* __restrict__ o,
                                       int h, int rq, char* smem) {
  const int tid = threadIdx.x, lane = tid & 63, wid = tid >> 6, fr = lane & 15, g = lane >> 4;
  float* kvs = (float*)smem;
  for (int i = tid; i < 48 * 32; i += 512) {
    float a = 0.f;
#pragma unroll
    for (int t = 0; t < 16; ++t) a += kvpart[((size_t)t * H + h) * (48 * 32) + i];
    kvs[i] = bfr(a);
  }
  __syncthreads();
  u32x4 ka[2][2];
#pragma unroll
  for (int j = 0; j < 2; ++j) {
    const int d = fr + 16 * j;
#pragma unroll
    for (int e = 0; e < 4; ++e) {
      ka[0][j][e] = pack2(kvs[(e < 2 ? 4 * g + 2 * e : 16 + 4 * g + 2 * (e - 2)) * 32 + d], kvs[(e < 2 ? 4 * g + 2 * e + 1 : 16 + 4 * g + 2 * (e - 2) + 1) * 32 + d]);
      ka[1][j][e] = (g == 0 && e < 2) ? pack2(kvs[(32 + 2 * e) * 32 + d], kvs[(33 + 2 * e) * 32 + d]) : 0u;
    }
  }
  u32x4 wa0, wa1;
  load_wa(wA, h, lane, wa0, wa1);
  u32x4 xq[8];
#pragma unroll
  for (int u = 0; u < 8; ++u) {
    const int row = rq * 1024 + wid * 128 + u * 16 + fr;
    const unsigned* qr = (const unsigned*)(qbuf + (size_t)row * D + h * DH + 8 * g);
    if (h == H - 1 && g >= 2) xq[u] = (g == 2) ? (u32x4){qr[0], 0u, 0u, 0u} : (u32x4){0u, 0u, 0u, 0u};
    else xq[u] = (u32x4){qr[0], qr[1], qr[2], qr[3]};
  }
#pragma unroll
  for (int u = 0; u < 8; ++u) {
    const int row = rq * 1024 + wid * 128 + u * 16 + fr;
    const u32x4 xb = xq[u];
    float e[8], ex[4];
    feat_mfma(xb, wa0, wa1, g == 0, e, ex);
    const u32x4 pb0 = {pack2(e[0], e[1]), pack2(e[2], e[3]), pack2(e[4], e[5]), pack2(e[6], e[7])};
    const u32x4 pb1 = {pack2(ex[0], ex[1]), pack2(ex[2], ex[3]), 0u, 0u};
    f32x4 n[2];
#pragma unroll
    for (int j = 0; j < 2; ++j) {
      f32x4 z = {0.f, 0.f, 0.f, 0.f};
      z = __builtin_amdgcn_mfma_f32_16x16x32_bf16(__builtin_bit_cast(bf16x8, ka[0][j]), __builtin_bit_cast(bf16x8, pb0), z, 0, 0, 0);
      n[j] = __builtin_amdgcn_mfma_f32_16x16x32_bf16(__builtin_bit_cast(bf16x8, ka[1][j]), __builtin_bit_cast(bf16x8, pb1), z, 0, 0, 0);
    }
    float den = g == 0 ? n[1][2] : 0.f;
    den = red16_32_add(den);
    den = bfr(bfr(den) + 1e-6f);
    const float rden = __builtin_amdgcn_rcpf(den);
    unsigned* orow = (unsigned*)(o + (size_t)row * D + h * DH);
    orow[2 * g] = pack2(bfr(n[0][0]) * rden, bfr(n[0][1]) * rden);
    orow[2 * g + 1] = pack2(bfr(n[0][2]) * rden, bfr(n[0][3]) * rden);
    if (g == 0) orow[8] = pack2(bfr(n[1][0]) * rden, bfr(n[1][1]) * rden);
  }
}

__global__ __launch_bounds__(512) void gemm2_kernel(const u16* __restrict__ h, const u16* __restrict__ w2, float* __restrict__ part,
                                                    const u16* __restrict__ qbuf, const u16* __restrict__ wA, const float* __restrict__ kvpart,
                                                    u16* __restrict__ o) {
  extern __shared__ __attribute__((aligned(16))) char smem[];
  const int tid = threadIdx.x, lane = tid & 63, wid = tid >> 6;
  if (blockIdx.x >= 192) {
    const int b = blockIdx.x - 192;
    o_step(qbuf, wA, kvpart, o, b >> 1, b & 1, smem);
    return;
  }
  const int fr = lane & 15, g = lane >> 4;
  const int split = blockIdx.x & 3, tile = blockIdx.x >> 2;
  const int tm = tile / 3, tn = tile % 3;
  const int row0 = tm * 128, col0 = tn * 192, k0 = split * 1152;
  f32x4 acc[2][6];
  gemm_core<4, 2, 2, 6, 3, 18, 0>(h + (size_t)row0 * F + k0, F, w2 + (size_t)k0 * D + col0, D, smem, acc);
  const int wm = wid >> 1, wn = wid & 1;
  u16* P = (u16*)part + (size_t)split * S * D;
#pragma unroll
  for (int mb = 0; mb < 2; ++mb) {
    const int m = row0 + wm * 32 + mb * 16 + fr;
#pragma unroll
    for (int nb = 0; nb < 6; ++nb) {
      const int n = col0 + wn * 96 + nb * 16 + 4 * g;
      *(u32x2*)(P + (size_t)m * D + n) = (u32x2){pack2(acc[mb][nb][0], acc[mb][nb][1]), pack2(acc[mb][nb][2], acc[mb][nb][3])};
    }
  }
}

__global__ __launch_bounds__(256) void final_kernel(const u16* __restrict__ x, const u16* __restrict__ o, const u16* __restrict__ wo,
                                                    const float* __restrict__ part, u16* __restrict__ out, int nsplit) {
  extern __shared__ __attribute__((aligned(16))) char smem[];
  const int tid = threadIdx.x, lane = tid & 63, wid = tid >> 6;
  const int fr = lane & 15, g = lane >> 4;
  const int tm = blockIdx.x / 9, tn = blockIdx.x % 9;
  const int row0 = tm * 64, col0 = tn * 64;
  const int wm = wid >> 1, wn = wid & 1;
  float res[2][2][4];
#pragma unroll
  for (int mb = 0; mb < 2; ++mb)
#pragma unroll
    for (int nb = 0; nb < 2; ++nb) {
      const int m = row0 + wm * 32 + mb * 16 + fr, n = col0 + wn * 32 + nb * 16 + 4 * g;
      f32x4 a = {0.f, 0.f, 0.f, 0.f};
#pragma unroll
      for (int sp = 0; sp < 4; ++sp) { const u32x2 p = *(const u32x2*)((const u16*)part + (size_t)sp * S * D + (size_t)m * D + n);
        a += (f32x4){__uint_as_float(p[0] << 16), __uint_as_float(p[0] & 0xffff0000u), __uint_as_float(p[1] << 16), __uint_as_float(p[1] & 0xffff0000u)}; }
      for (int r = 0; r < 4; ++r) res[mb][nb][r] = a[r];
    }
#pragma unroll
  for (int mb = 0; mb < 2; ++mb)
#pragma unroll
    for (int nb = 0; nb < 2; ++nb)
      for (int r = 0; r < 4; ++r) asm volatile("" : "+v"(res[mb][nb][r]));
  f32x4 acc[2][2];
  gemm_core<2, 2, 2, 2, 1, 9, 0>(o + (size_t)row0 * D, D, wo + col0, D, smem, acc);
#pragma unroll
  for (int mb = 0; mb < 2; ++mb)
#pragma unroll
    for (int nb = 0; nb < 2; ++nb) {
      const int m = row0 + wm * 32 + mb * 16 + fr, n = col0 + wn * 32 + nb * 16 + 4 * g;
      u32x2 xv = *(const u32x2*)(x + (size_t)m * D + n);
      float xs[4] = {__uint_as_float(xv[0] << 16), __uint_as_float(xv[0] & 0xffff0000u), __uint_as_float(xv[1] << 16), __uint_as_float(xv[1] & 0xffff0000u)};
      float v[4];
      for (int r = 0; r < 4; ++r) v[r] = bfr(xs[r] + bfr(acc[mb][nb][r])) + bfr(res[mb][nb][r]);
      u32x2 w = {pack2(v[0], v[1]), pack2(v[2], v[3])};
      *(u32x2*)(out + (size_t)m * D + n) = w;
    }
}

extern "C" void kernel_launch(void* const* d_in, const int* in_sizes, int n_in, void* d_out, int out_size, void* d_ws, size_t ws_size,
                              hipStream_t stream) {
  const u16* x = (const u16*)d_in[0];
  const u16* wqkv = (const u16*)d_in[1];
  const u16* wphi = (const u16*)d_in[2];
  const u16* wo = (const u16*)d_in[3];
  const u16* w1 = (const u16*)d_in[5];
  const u16* w2 = (const u16*)d_in[6];
  u16* out = (u16*)d_out;
  char* ws = (char*)d_ws;
  size_t off = 0;
  auto take = [&](size_t bytes) { char* p = ws + off; off += (bytes + 255) & ~(size_t)255; return p; };
  u16* qbuf = (u16*)take((size_t)S * D * 2);
  u16* h = (u16*)take((size_t)S * F * 2);
  u16* o = (u16*)take((size_t)S * D * 2);
  float* kvpart = (float*)take((size_t)16 * H * 48 * 32 * 4);
  const u16* wA = wphi;
  const u16* xn = x;
  constexpr int NSPLIT = 4;
  float* part = (float*)take((size_t)NSPLIT * S * D * 4);

  const int shm1 = 160 * 1024, shm2 = (3 * 128 + 2 * 192) * 128;
  hipFuncSetAttribute((const void*)gemm1_kernel, hipFuncAttributeMaxDynamicSharedMemorySize, shm1);
  hipFuncSetAttribute((const void*)gemm2_kernel, hipFuncAttributeMaxDynamicSharedMemorySize, shm2);
  gemm1_kernel<<<256, 512, shm1, stream>>>(xn, w1, wqkv, wA, h, qbuf, kvpart, w2, wo);
  gemm2_kernel<<<192 + 64, 512, shm2, stream>>>(h, w2, part, qbuf, wA, kvpart, o);
  final_kernel<<<32 * 9, 256, (3 * 64 + 2 * 64) * 128, stream>>>(x, o, wo, part, out, NSPLIT);
}
```
